```python
import torch

def _softmax(x, axis=-1):
    return torch.softmax(x, dim=axis)
def _gelu(x):
    return torch.nn.functional.gelu(x, approximate="tanh")
def _silu(x):
    return torch.nn.functional.silu(x)
def _sigmoid(x):
    return torch.sigmoid(x)
def _layernorm(x, axis=-1, eps=1e-5):
    m = x.mean(dim=axis, keepdim=True)
    v = x.var(dim=axis, keepdim=True, unbiased=False)
    return (x - m) / torch.sqrt(v + eps)
def _scatter_add(tgt, idx, src, axis=0):
    return tgt.clone().index_add_(axis, idx, src)
def _segment_sum(src, seg, n):
    out = torch.zeros((n,) + src.shape[1:], dtype=src.dtype)
    return out.index_add_(0, seg, src)
def _pad(x, pw):
    flat = tuple(v for pair in reversed(pw) for v in pair)
    return torch.nn.functional.pad(x, flat)

kernel_name = 'interleave_layers__softmax_full_mha__glu_gated_reglu__prompt'

def setup_inputs():

    _w = (lambda a: (lambda t: t.to(torch.bfloat16) if t.is_floating_point() else t)(torch.as_tensor(a)))
    def _A_setup():
        B, S, D = 1, 2048, 640
        return {
            "x": _w((lambda *s: torch.randn(*s, dtype=torch.bfloat16) * 0.05)(B, S, D)),
            "wqkv": _w((lambda *s: torch.randn(*s, dtype=torch.bfloat16) * 0.05)(D, 3 * D)),
            "wo": _w((lambda *s: torch.randn(*s, dtype=torch.bfloat16) * 0.05)(D, D)),
        }
    _A_in = _A_setup()

    def _B_setup():
        B, S, D, F = 1, 2048, 640, 1536
        return {
            "x": _w((lambda *s: torch.randn(*s, dtype=torch.bfloat16) * 0.05)(B, S, D)),
            "W_gate": _w((lambda *s: torch.randn(*s, dtype=torch.bfloat16) * 0.8)(D, F) * (2.0 / D) ** 0.5),
            "W_up": _w((lambda *s: torch.randn(*s, dtype=torch.bfloat16) * 0.8)(D, F) * (2.0 / D) ** 0.5),
            "W_down": _w((lambda *s: torch.randn(*s, dtype=torch.bfloat16) * 0.8)(F, D) * (1.0 / F) ** 0.5),
        }
    _B_in = _B_setup()

    return {
        "A_x": _A_in["x"],
        "A_wqkv": _A_in["wqkv"],
        "A_wo": _A_in["wo"],
        "B_x": _B_in["x"],
        "B_W_gate": _B_in["W_gate"],
        "B_W_up": _B_in["W_up"],
        "B_W_down": _B_in["W_down"],
    }

def reference(A_x, A_wqkv, A_wo, B_x, B_W_gate, B_W_up, B_W_down):
    def _A_ref(x, wqkv, wo):
        B, S, D = x.shape
        H = 32; Dh = D // H
        qkv = (lambda x, *d: x.permute(*d))((x @ wqkv).reshape(B, S, 3, H, Dh), 2, 0, 3, 1, 4)
        q, k, v = qkv[0], qkv[1], qkv[2]
        scale = Dh ** -0.5
        scores = torch.einsum("bhqd,bhkd->bhqk", q, k) * scale
        p = _softmax(scores, axis=-1)
        o = torch.einsum("bhqk,bhkd->bhqd", p, v)
        return (lambda x, *d: x.permute(*d))(o, 0, 2, 1, 3).reshape(B, S, D) @ wo

    def _B_ref(x, W_gate, W_up, W_down):
        gate = torch.einsum("bsd,df->bsf", x, W_gate)
        up = torch.einsum("bsd,df->bsf", x, W_up)
        gate = gate * (lambda x, dt: x.to(dt))(gate > 0, gate.dtype)
        h = gate * up
        return torch.einsum("bsf,fd->bsd", h, W_down)

    x = A_x
    x = x + _A_ref(_layernorm(x, axis=-1), A_wqkv, A_wo)
    x = x + _B_ref(_layernorm(x, axis=-1), B_W_gate, B_W_up, B_W_down)
    x = x + _A_ref(_layernorm(x, axis=-1), A_wqkv, A_wo)
    x = x + _B_ref(_layernorm(x, axis=-1), B_W_gate, B_W_up, B_W_down)
    return x
```

```cpp
#include <hip/hip_runtime.h>
#include <hip/hip_bf16.h>
#include <stdint.h>

typedef float    f32x4  __attribute__((ext_vector_type(4)));
typedef float    f32x2  __attribute__((ext_vector_type(2)));
typedef float    f32x16 __attribute__((ext_vector_type(16)));
typedef unsigned u32x4  __attribute__((ext_vector_type(4)));
typedef unsigned u32x2  __attribute__((ext_vector_type(2)));
typedef short    s16x4  __attribute__((ext_vector_type(4)));
typedef __bf16   bf16x8 __attribute__((ext_vector_type(8)));
typedef __bf16   bf16x2 __attribute__((ext_vector_type(2)));
typedef unsigned long long u64;
typedef __amdgpu_buffer_rsrc_t rsrc_t;
#define LAS __attribute__((address_space(3)))
#define GAS __attribute__((address_space(1)))
#define DI __device__ __forceinline__
#define RLX_AGENT __ATOMIC_RELAXED, __HIP_MEMORY_SCOPE_AGENT
constexpr int SC1 = 16;

#define ATTN_ASM \
  "v_mov_b32 v175, 0x80000000\n\t" \
  "s_nop 3\n\t" \
  "s_add_u32 m0, %[lds], 0\n\t" \
  "s_nop 0\n\t" \
  "buffer_load_dwordx4 v173, %[srd], %[so] offen sc1 lds\n\t" \
  "s_mov_b64 exec, 0xfffff\n\t" \
  "buffer_load_dwordx4 v173, %[srd], %[so] offen offset:1024 sc1 lds\n\t" \
  "s_mov_b64 exec, -1\n\t" \
  "s_add_u32 %[so], %[so], 10752\n\t" \
  "buffer_load_dwordx4 v[128:131], v96, %[srd], %[q0] offen sc1\n\t" \
  "buffer_load_dwordx2 v[132:133], v97, %[srd], %[q0] offen sc1\n\t" \
  "buffer_load_dwordx4 v[134:137], v96, %[srd], %[q1] offen sc1\n\t" \
  "buffer_load_dwordx2 v[138:139], v97, %[srd], %[q1] offen sc1\n\t" \
  "v_mov_b32 v168, 0xdddddddd\n\t" \
  "s_mov_b32 %[trip], 0\n\t" \
  "s_mov_b32 vcc_lo, 0\n\t" \
  "s_mov_b32 vcc_hi, -1\n\t" \
  "s_waitcnt vmcnt(0)\n\t" \
  "s_barrier\n\t" \
  "ds_read_b64 v[164:165], v99\n\t" \
  "ds_read_b128 v[160:163], v98\n\t" \
  "ds_read_b64 v[144:145], v170 offset:0\n\t" \
  "ds_read_b128 v[140:143], v169 offset:0\n\t" \
  "ds_read_b128 v[146:149], v169 offset:2688\n\t" \
  "ds_read_b64 v[150:151], v170 offset:2688\n\t" \
  "ds_read_b128 v[152:155], v171 offset:0\n\t" \
  "ds_read_b128 v[156:159], v172 offset:0\n\t" \
  "s_cmp_eq_u32 %[late], 0\n\t" \
  "s_cbranch_scc1 Lattn_nl%=\n\t" \
  "s_mov_b64 exec, 1\n\t" \
  "buffer_store_dwordx2 v[102:103], v104, %[srd], 0 offen sc1\n\t" \
  "s_mov_b64 exec, -1\n\t" \
  "Lattn_nl%=:\n\t" \
  "s_cmp_lg_u32 %[again], 0\n\t" \
  "s_cbranch_scc1 Lattn_hv0%=\n\t" \
  "s_waitcnt lgkmcnt(7)\n\t" \
  "v_mfma_f32_32x32x8_bf16 v[32:47], v[164:165], v[132:133], 0\n\t" \
  "s_waitcnt lgkmcnt(6)\n\t" \
  "v_mfma_f32_32x32x16_bf16 v[32:47], v[160:163], v[128:131], v[32:47]\n\t" \
  "v_mfma_f32_32x32x8_bf16 v[48:63], v[164:165], v[138:139], 0\n\t" \
  "v_mfma_f32_32x32x16_bf16 v[48:63], v[160:163], v[134:137], v[48:63]\n\t" \
  "Lattn_hv0%=:\n\t" \
  "s_add_u32 m0, %[lds], 10752\n\t" \
  "s_nop 0\n\t" \
  "buffer_load_dwordx4 v173, %[srd], %[so] offen sc1 lds\n\t" \
  "s_mov_b64 exec, 0xfffff\n\t" \
  "buffer_load_dwordx4 v173, %[srd], %[so] offen offset:1024 sc1 lds\n\t" \
  "s_mov_b64 exec, -1\n\t" \
  "s_add_u32 %[so], %[so], 10752\n\t" \
  "s_add_u32 m0, %[lds], 21504\n\t" \
  "s_nop 0\n\t" \
  "buffer_load_dwordx4 v173, %[srd], %[so] offen sc1 lds\n\t" \
  "s_mov_b64 exec, 0xfffff\n\t" \
  "buffer_load_dwordx4 v173, %[srd], %[so] offen offset:1024 sc1 lds\n\t" \
  "s_mov_b64 exec, -1\n\t" \
  "s_add_u32 %[so], %[so], 10752\n\t" \
  "s_mov_b32 m0, %[junk]\n\t" \
  "s_nop 0\n\t" \
  "buffer_load_dword v175, %[srd], 0 offen sc1 lds\n\t" \
  "s_cmp_lg_u32 %[again], 0\n\t" \
  "s_cbranch_scc1 Lattn_hv1%=\n\t" \
  "s_nop 7\n\t" \
  "v_max3_f32 v100, v32, v33, v34\n\t" \
  "v_max3_f32 v105, v35, v36, v37\n\t" \
  "v_max3_f32 v106, v38, v39, v40\n\t" \
  "v_max3_f32 v107, v41, v42, v43\n\t" \
  "v_max3_f32 v100, v100, v44, v45\n\t" \
  "v_max3_f32 v105, v105, v46, v47\n\t" \
  "v_max3_f32 v100, v100, v106, v107\n\t" \
  "v_max_f32 v100, v100, v105\n\t" \
  "v_mov_b32 v105, v100\n\t" \
  "s_nop 1\n\t" \
  "v_permlane32_swap_b32 v100, v105\n\t" \
  "v_min_f32 v100, -v100, -v105\n\t" \
  "Lattn_hv1%=:\n\t" \
  "v_and_b32 v105, 0xffff0000, v100\n\t" \
  "v_sub_f32 v106, v100, v105\n\t" \
  "v_lshrrev_b32 v105, 16, v105\n\t" \
  "v_and_b32 v107, 0xffff0000, v106\n\t" \
  "v_sub_f32 v106, v106, v107\n\t" \
  "v_or_b32 v105, v105, v107\n\t" \
  "v_lshrrev_b32 v106, 16, v106\n\t" \
  "v_cndmask_b32 v132, v132, v105, vcc\n\t" \
  "v_cndmask_b32 v133, v133, v106, vcc\n\t" \
  "s_waitcnt lgkmcnt(4)\n\t" \
  "v_mov_b32 v164, 0\n\t" \
  "v_mfma_f32_32x32x8_bf16 v[0:15], v[144:145], v[132:133], 0\n\t" \
  "v_mov_b32 v165, 0\n\t" \
  "v_mfma_f32_32x32x16_bf16 v[0:15], v[140:143], v[128:131], v[0:15]\n\t" \
  "s_cbranch_scc1 Lattn_hv2%=\n\t" \
  "v_max3_f32 v101, v48, v49, v50\n\t" \
  "v_max3_f32 v105, v51, v52, v53\n\t" \
  "v_max3_f32 v106, v54, v55, v56\n\t" \
  "v_max3_f32 v107, v57, v58, v59\n\t" \
  "v_max3_f32 v101, v101, v60, v61\n\t" \
  "v_max3_f32 v105, v105, v62, v63\n\t" \
  "v_max3_f32 v101, v101, v106, v107\n\t" \
  "v_max_f32 v101, v101, v105\n\t" \
  "v_mov_b32 v105, v101\n\t" \
  "s_nop 1\n\t" \
  "v_permlane32_swap_b32 v101, v105\n\t" \
  "v_min_f32 v101, -v101, -v105\n\t" \
  "Lattn_hv2%=:\n\t" \
  "v_and_b32 v105, 0xffff0000, v101\n\t" \
  "v_sub_f32 v106, v101, v105\n\t" \
  "v_lshrrev_b32 v105, 16, v105\n\t" \
  "v_and_b32 v107, 0xffff0000, v106\n\t" \
  "v_sub_f32 v106, v106, v107\n\t" \
  "v_or_b32 v105, v105, v107\n\t" \
  "v_lshrrev_b32 v106, 16, v106\n\t" \
  "v_cndmask_b32 v138, v138, v105, vcc\n\t" \
  "v_cndmask_b32 v139, v139, v106, vcc\n\t" \
  "v_mov_b32 v166, 0\n\t" \
  "v_mov_b32 v167, 0\n\t" \
  "v_mfma_f32_32x32x8_bf16 v[16:31], v[144:145], v[138:139], 0\n\t" \
  "v_mov_b32 v56, 0\n\t" \
  "v_mov_b32 v57, 0\n\t" \
  "v_mov_b32 v58, 0\n\t" \
  "v_mov_b32 v59, 0\n\t" \
  "v_mfma_f32_32x32x16_bf16 v[16:31], v[140:143], v[134:137], v[16:31]\n\t" \
  "v_mov_b32 v60, 0\n\t" \
  "v_mov_b32 v61, 0\n\t" \
  "v_mov_b32 v62, 0\n\t" \
  "v_mov_b32 v63, 0\n\t" \
  "Lattn_loop%=:\n\t" \
  "s_waitcnt vmcnt(3) lgkmcnt(0)\n\t" \
  "s_barrier\n\t" \
  "s_setprio 3\n\t" \
  "v_exp_f32 v0, v0\n\t" \
  "v_exp_f32 v1, v1\n\t" \
  "v_smfmac_f32_32x32x32_bf16 v[80:95], v[164:167], v[56:63], v168\n\t" \
  "ds_read_b128 v[140:143], v169 offset:10752\n\t" \
  "v_exp_f32 v2, v2\n\t" \
  "v_exp_f32 v3, v3\n\t" \
  "v_exp_f32 v4, v4\n\t" \
  "v_exp_f32 v5, v5\n\t" \
  "v_mfma_f32_32x32x8_bf16 v[32:47], v[150:151], v[132:133], 0\n\t" \
  "ds_read_b64 v[144:145], v170 offset:10752\n\t" \
  "v_exp_f32 v6, v6\n\t" \
  "v_exp_f32 v7, v7\n\t" \
  "v_exp_f32 v8, v8\n\t" \
  "v_exp_f32 v9, v9\n\t" \
  "v_smfmac_f32_32x32x32_bf16 v[64:79], v[152:155], v[0:7], v168\n\t" \
  "ds_read_b128 v[160:163], v171 offset:2688\n\t" \
  "v_exp_f32 v10, v10\n\t" \
  "v_exp_f32 v11, v11\n\t" \
  "v_exp_f32 v12, v12\n\t" \
  "v_exp_f32 v13, v13\n\t" \
  "v_mfma_f32_32x32x16_bf16 v[32:47], v[146:149], v[128:131], v[32:47]\n\t" \
  "ds_read_b128 v[164:167], v172 offset:2688\n\t" \
  "v_exp_f32 v14, v14\n\t" \
  "v_exp_f32 v15, v15\n\t" \
  "v_exp_f32 v16, v16\n\t" \
  "v_exp_f32 v17, v17\n\t" \
  "v_smfmac_f32_32x32x32_bf16 v[64:79], v[156:159], v[8:15], v168\n\t" \
  "s_setprio 2\n\t" \
  "s_add_u32 m0, %[lds], 32256\n\t" \
  "s_nop 0\n\t" \
  "buffer_load_dwordx4 v173, %[srd], %[so] offen sc1 lds\n\t" \
  "s_mov_b64 exec, 0xfffff\n\t" \
  "buffer_load_dwordx4 v173, %[srd], %[so] offen offset:1024 sc1 lds\n\t" \
  "s_mov_b64 exec, -1\n\t" \
  "s_add_u32 %[so], %[so], 10752\n\t" \
  "v_exp_f32 v18, v18\n\t" \
  "v_exp_f32 v19, v19\n\t" \
  "v_exp_f32 v20, v20\n\t" \
  "v_exp_f32 v21, v21\n\t" \
  "v_mfma_f32_32x32x8_bf16 v[48:63], v[150:151], v[138:139], 0\n\t" \
  "v_exp_f32 v22, v22\n\t" \
  "v_exp_f32 v23, v23\n\t" \
  "v_exp_f32 v24, v24\n\t" \
  "v_exp_f32 v25, v25\n\t" \
  "v_smfmac_f32_32x32x32_bf16 v[80:95], v[152:155], v[16:23], v168\n\t" \
  "v_exp_f32 v26, v26\n\t" \
  "v_exp_f32 v27, v27\n\t" \
  "v_exp_f32 v28, v28\n\t" \
  "v_exp_f32 v29, v29\n\t" \
  "v_mfma_f32_32x32x16_bf16 v[48:63], v[146:149], v[134:137], v[48:63]\n\t" \
  "v_exp_f32 v30, v30\n\t" \
  "v_exp_f32 v31, v31\n\t" \
  "s_waitcnt lgkmcnt(0)\n\t" \
  "s_setprio 1\n\t" \
  "v_exp_f32 v32, v32\n\t" \
  "v_exp_f32 v33, v33\n\t" \
  "v_smfmac_f32_32x32x32_bf16 v[80:95], v[156:159], v[24:31], v168\n\t" \
  "ds_read_b128 v[146:149], v169 offset:13440\n\t" \
  "v_exp_f32 v34, v34\n\t" \
  "v_exp_f32 v35, v35\n\t" \
  "v_exp_f32 v36, v36\n\t" \
  "v_exp_f32 v37, v37\n\t" \
  "v_mfma_f32_32x32x8_bf16 v[0:15], v[144:145], v[132:133], 0\n\t" \
  "ds_read_b64 v[150:151], v170 offset:13440\n\t" \
  "v_exp_f32 v38, v38\n\t" \
  "v_exp_f32 v39, v39\n\t" \
  "v_exp_f32 v40, v40\n\t" \
  "v_exp_f32 v41, v41\n\t" \
  "v_smfmac_f32_32x32x32_bf16 v[64:79], v[160:163], v[32:39], v168\n\t" \
  "ds_read_b128 v[152:155], v171 offset:10752\n\t" \
  "v_exp_f32 v42, v42\n\t" \
  "v_exp_f32 v43, v43\n\t" \
  "v_exp_f32 v44, v44\n\t" \
  "v_exp_f32 v45, v45\n\t" \
  "v_mfma_f32_32x32x16_bf16 v[0:15], v[140:143], v[128:131], v[0:15]\n\t" \
  "ds_read_b128 v[156:159], v172 offset:10752\n\t" \
  "v_exp_f32 v46, v46\n\t" \
  "v_exp_f32 v47, v47\n\t" \
  "v_exp_f32 v48, v48\n\t" \
  "v_exp_f32 v49, v49\n\t" \
  "v_smfmac_f32_32x32x32_bf16 v[64:79], v[164:167], v[40:47], v168\n\t" \
  "s_setprio 0\n\t" \
  "s_mov_b32 m0, %[wl]\n\t" \
  "s_add_u32 %[wl], %[wl], 1024\n\t" \
  "buffer_load_dwordx4 v96, %[srd], %[ws] offen sc1 lds\n\t" \
  "s_add_u32 %[ws], %[ws], 1024\n\t" \
  "v_exp_f32 v50, v50\n\t" \
  "v_exp_f32 v51, v51\n\t" \
  "v_exp_f32 v52, v52\n\t" \
  "v_exp_f32 v53, v53\n\t" \
  "v_mfma_f32_32x32x8_bf16 v[16:31], v[144:145], v[138:139], 0\n\t" \
  "v_exp_f32 v54, v54\n\t" \
  "v_exp_f32 v55, v55\n\t" \
  "v_exp_f32 v56, v56\n\t" \
  "v_exp_f32 v57, v57\n\t" \
  "v_smfmac_f32_32x32x32_bf16 v[80:95], v[160:163], v[48:55], v168\n\t" \
  "v_exp_f32 v58, v58\n\t" \
  "v_exp_f32 v59, v59\n\t" \
  "v_exp_f32 v60, v60\n\t" \
  "v_exp_f32 v61, v61\n\t" \
  "v_mfma_f32_32x32x16_bf16 v[16:31], v[140:143], v[134:137], v[16:31]\n\t" \
  "v_exp_f32 v62, v62\n\t" \
  "v_exp_f32 v63, v63\n\t" \
  "s_waitcnt vmcnt(3) lgkmcnt(0)\n\t" \
  "s_barrier\n\t" \
  "s_setprio 3\n\t" \
  "v_exp_f32 v0, v0\n\t" \
  "v_exp_f32 v1, v1\n\t" \
  "v_smfmac_f32_32x32x32_bf16 v[80:95], v[164:167], v[56:63], v168\n\t" \
  "ds_read_b128 v[140:143], v169 offset:21504\n\t" \
  "v_exp_f32 v2, v2\n\t" \
  "v_exp_f32 v3, v3\n\t" \
  "v_exp_f32 v4, v4\n\t" \
  "v_exp_f32 v5, v5\n\t" \
  "v_mfma_f32_32x32x8_bf16 v[32:47], v[150:151], v[132:133], 0\n\t" \
  "ds_read_b64 v[144:145], v170 offset:21504\n\t" \
  "v_exp_f32 v6, v6\n\t" \
  "v_exp_f32 v7, v7\n\t" \
  "v_exp_f32 v8, v8\n\t" \
  "v_exp_f32 v9, v9\n\t" \
  "v_smfmac_f32_32x32x32_bf16 v[64:79], v[152:155], v[0:7], v168\n\t" \
  "ds_read_b128 v[160:163], v171 offset:13440\n\t" \
  "v_exp_f32 v10, v10\n\t" \
  "v_exp_f32 v11, v11\n\t" \
  "v_exp_f32 v12, v12\n\t" \
  "v_exp_f32 v13, v13\n\t" \
  "v_mfma_f32_32x32x16_bf16 v[32:47], v[146:149], v[128:131], v[32:47]\n\t" \
  "ds_read_b128 v[164:167], v172 offset:13440\n\t" \
  "v_exp_f32 v14, v14\n\t" \
  "v_exp_f32 v15, v15\n\t" \
  "v_exp_f32 v16, v16\n\t" \
  "v_exp_f32 v17, v17\n\t" \
  "v_smfmac_f32_32x32x32_bf16 v[64:79], v[156:159], v[8:15], v168\n\t" \
  "s_setprio 2\n\t" \
  "s_add_u32 m0, %[lds], 0\n\t" \
  "s_nop 0\n\t" \
  "buffer_load_dwordx4 v173, %[srd], %[so] offen sc1 lds\n\t" \
  "s_mov_b64 exec, 0xfffff\n\t" \
  "buffer_load_dwordx4 v173, %[srd], %[so] offen offset:1024 sc1 lds\n\t" \
  "s_mov_b64 exec, -1\n\t" \
  "s_add_u32 %[so], %[so], 10752\n\t" \
  "v_exp_f32 v18, v18\n\t" \
  "v_exp_f32 v19, v19\n\t" \
  "v_exp_f32 v20, v20\n\t" \
  "v_exp_f32 v21, v21\n\t" \
  "v_mfma_f32_32x32x8_bf16 v[48:63], v[150:151], v[138:139], 0\n\t" \
  "v_exp_f32 v22, v22\n\t" \
  "v_exp_f32 v23, v23\n\t" \
  "v_exp_f32 v24, v24\n\t" \
  "v_exp_f32 v25, v25\n\t" \
  "v_smfmac_f32_32x32x32_bf16 v[80:95], v[152:155], v[16:23], v168\n\t" \
  "v_exp_f32 v26, v26\n\t" \
  "v_exp_f32 v27, v27\n\t" \
  "v_exp_f32 v28, v28\n\t" \
  "v_exp_f32 v29, v29\n\t" \
  "v_mfma_f32_32x32x16_bf16 v[48:63], v[146:149], v[134:137], v[48:63]\n\t" \
  "v_exp_f32 v30, v30\n\t" \
  "v_exp_f32 v31, v31\n\t" \
  "s_waitcnt lgkmcnt(0)\n\t" \
  "s_setprio 1\n\t" \
  "v_exp_f32 v32, v32\n\t" \
  "v_exp_f32 v33, v33\n\t" \
  "v_smfmac_f32_32x32x32_bf16 v[80:95], v[156:159], v[24:31], v168\n\t" \
  "ds_read_b128 v[146:149], v169 offset:24192\n\t" \
  "v_exp_f32 v34, v34\n\t" \
  "v_exp_f32 v35, v35\n\t" \
  "v_exp_f32 v36, v36\n\t" \
  "v_exp_f32 v37, v37\n\t" \
  "v_mfma_f32_32x32x8_bf16 v[0:15], v[144:145], v[132:133], 0\n\t" \
  "ds_read_b64 v[150:151], v170 offset:24192\n\t" \
  "v_exp_f32 v38, v38\n\t" \
  "v_exp_f32 v39, v39\n\t" \
  "v_exp_f32 v40, v40\n\t" \
  "v_exp_f32 v41, v41\n\t" \
  "v_smfmac_f32_32x32x32_bf16 v[64:79], v[160:163], v[32:39], v168\n\t" \
  "ds_read_b128 v[152:155], v171 offset:21504\n\t" \
  "v_exp_f32 v42, v42\n\t" \
  "v_exp_f32 v43, v43\n\t" \
  "v_exp_f32 v44, v44\n\t" \
  "v_exp_f32 v45, v45\n\t" \
  "v_mfma_f32_32x32x16_bf16 v[0:15], v[140:143], v[128:131], v[0:15]\n\t" \
  "ds_read_b128 v[156:159], v172 offset:21504\n\t" \
  "v_exp_f32 v46, v46\n\t" \
  "v_exp_f32 v47, v47\n\t" \
  "v_exp_f32 v48, v48\n\t" \
  "v_exp_f32 v49, v49\n\t" \
  "v_smfmac_f32_32x32x32_bf16 v[64:79], v[164:167], v[40:47], v168\n\t" \
  "s_setprio 0\n\t" \
  "s_mov_b32 m0, %[wl]\n\t" \
  "s_add_u32 %[wl], %[wl], 1024\n\t" \
  "buffer_load_dwordx4 v96, %[srd], %[ws] offen sc1 lds\n\t" \
  "s_add_u32 %[ws], %[ws], 1024\n\t" \
  "v_exp_f32 v50, v50\n\t" \
  "v_exp_f32 v51, v51\n\t" \
  "v_exp_f32 v52, v52\n\t" \
  "v_exp_f32 v53, v53\n\t" \
  "v_mfma_f32_32x32x8_bf16 v[16:31], v[144:145], v[138:139], 0\n\t" \
  "v_exp_f32 v54, v54\n\t" \
  "v_exp_f32 v55, v55\n\t" \
  "v_exp_f32 v56, v56\n\t" \
  "v_exp_f32 v57, v57\n\t" \
  "v_smfmac_f32_32x32x32_bf16 v[80:95], v[160:163], v[48:55], v168\n\t" \
  "v_exp_f32 v58, v58\n\t" \
  "v_exp_f32 v59, v59\n\t" \
  "v_exp_f32 v60, v60\n\t" \
  "v_exp_f32 v61, v61\n\t" \
  "v_mfma_f32_32x32x16_bf16 v[16:31], v[140:143], v[134:137], v[16:31]\n\t" \
  "v_exp_f32 v62, v62\n\t" \
  "v_exp_f32 v63, v63\n\t" \
  "s_waitcnt vmcnt(3) lgkmcnt(0)\n\t" \
  "s_barrier\n\t" \
  "s_setprio 3\n\t" \
  "v_exp_f32 v0, v0\n\t" \
  "v_exp_f32 v1, v1\n\t" \
  "v_smfmac_f32_32x32x32_bf16 v[80:95], v[164:167], v[56:63], v168\n\t" \
  "ds_read_b128 v[140:143], v169 offset:32256\n\t" \
  "v_exp_f32 v2, v2\n\t" \
  "v_exp_f32 v3, v3\n\t" \
  "v_exp_f32 v4, v4\n\t" \
  "v_exp_f32 v5, v5\n\t" \
  "v_mfma_f32_32x32x8_bf16 v[32:47], v[150:151], v[132:133], 0\n\t" \
  "ds_read_b64 v[144:145], v170 offset:32256\n\t" \
  "v_exp_f32 v6, v6\n\t" \
  "v_exp_f32 v7, v7\n\t" \
  "v_exp_f32 v8, v8\n\t" \
  "v_exp_f32 v9, v9\n\t" \
  "v_smfmac_f32_32x32x32_bf16 v[64:79], v[152:155], v[0:7], v168\n\t" \
  "ds_read_b128 v[160:163], v171 offset:24192\n\t" \
  "v_exp_f32 v10, v10\n\t" \
  "v_exp_f32 v11, v11\n\t" \
  "v_exp_f32 v12, v12\n\t" \
  "v_exp_f32 v13, v13\n\t" \
  "v_mfma_f32_32x32x16_bf16 v[32:47], v[146:149], v[128:131], v[32:47]\n\t" \
  "ds_read_b128 v[164:167], v172 offset:24192\n\t" \
  "v_exp_f32 v14, v14\n\t" \
  "v_exp_f32 v15, v15\n\t" \
  "v_exp_f32 v16, v16\n\t" \
  "v_exp_f32 v17, v17\n\t" \
  "v_smfmac_f32_32x32x32_bf16 v[64:79], v[156:159], v[8:15], v168\n\t" \
  "s_setprio 2\n\t" \
  "s_add_u32 m0, %[lds], 10752\n\t" \
  "s_nop 0\n\t" \
  "buffer_load_dwordx4 v173, %[srd], %[so] offen sc1 lds\n\t" \
  "s_mov_b64 exec, 0xfffff\n\t" \
  "buffer_load_dwordx4 v173, %[srd], %[so] offen offset:1024 sc1 lds\n\t" \
  "s_mov_b64 exec, -1\n\t" \
  "s_add_u32 %[so], %[so], 10752\n\t" \
  "v_exp_f32 v18, v18\n\t" \
  "v_exp_f32 v19, v19\n\t" \
  "v_exp_f32 v20, v20\n\t" \
  "v_exp_f32 v21, v21\n\t" \
  "v_mfma_f32_32x32x8_bf16 v[48:63], v[150:151], v[138:139], 0\n\t" \
  "v_exp_f32 v22, v22\n\t" \
  "v_exp_f32 v23, v23\n\t" \
  "v_exp_f32 v24, v24\n\t" \
  "v_exp_f32 v25, v25\n\t" \
  "v_smfmac_f32_32x32x32_bf16 v[80:95], v[152:155], v[16:23], v168\n\t" \
  "v_exp_f32 v26, v26\n\t" \
  "v_exp_f32 v27, v27\n\t" \
  "v_exp_f32 v28, v28\n\t" \
  "v_exp_f32 v29, v29\n\t" \
  "v_mfma_f32_32x32x16_bf16 v[48:63], v[146:149], v[134:137], v[48:63]\n\t" \
  "v_exp_f32 v30, v30\n\t" \
  "v_exp_f32 v31, v31\n\t" \
  "s_waitcnt lgkmcnt(0)\n\t" \
  "s_setprio 1\n\t" \
  "v_exp_f32 v32, v32\n\t" \
  "v_exp_f32 v33, v33\n\t" \
  "v_smfmac_f32_32x32x32_bf16 v[80:95], v[156:159], v[24:31], v168\n\t" \
  "ds_read_b128 v[146:149], v169 offset:34944\n\t" \
  "v_exp_f32 v34, v34\n\t" \
  "v_exp_f32 v35, v35\n\t" \
  "v_exp_f32 v36, v36\n\t" \
  "v_exp_f32 v37, v37\n\t" \
  "v_mfma_f32_32x32x8_bf16 v[0:15], v[144:145], v[132:133], 0\n\t" \
  "ds_read_b64 v[150:151], v170 offset:34944\n\t" \
  "v_exp_f32 v38, v38\n\t" \
  "v_exp_f32 v39, v39\n\t" \
  "v_exp_f32 v40, v40\n\t" \
  "v_exp_f32 v41, v41\n\t" \
  "v_smfmac_f32_32x32x32_bf16 v[64:79], v[160:163], v[32:39], v168\n\t" \
  "ds_read_b128 v[152:155], v171 offset:32256\n\t" \
  "v_exp_f32 v42, v42\n\t" \
  "v_exp_f32 v43, v43\n\t" \
  "v_exp_f32 v44, v44\n\t" \
  "v_exp_f32 v45, v45\n\t" \
  "v_mfma_f32_32x32x16_bf16 v[0:15], v[140:143], v[128:131], v[0:15]\n\t" \
  "ds_read_b128 v[156:159], v172 offset:32256\n\t" \
  "v_exp_f32 v46, v46\n\t" \
  "v_exp_f32 v47, v47\n\t" \
  "v_exp_f32 v48, v48\n\t" \
  "v_exp_f32 v49, v49\n\t" \
  "v_smfmac_f32_32x32x32_bf16 v[64:79], v[164:167], v[40:47], v168\n\t" \
  "s_setprio 0\n\t" \
  "s_mov_b32 m0, %[wl]\n\t" \
  "s_add_u32 %[wl], %[wl], 1024\n\t" \
  "buffer_load_dwordx4 v96, %[srd], %[ws] offen sc1 lds\n\t" \
  "s_add_u32 %[ws], %[ws], 1024\n\t" \
  "v_exp_f32 v50, v50\n\t" \
  "v_exp_f32 v51, v51\n\t" \
  "v_exp_f32 v52, v52\n\t" \
  "v_exp_f32 v53, v53\n\t" \
  "v_mfma_f32_32x32x8_bf16 v[16:31], v[144:145], v[138:139], 0\n\t" \
  "v_exp_f32 v54, v54\n\t" \
  "v_exp_f32 v55, v55\n\t" \
  "v_exp_f32 v56, v56\n\t" \
  "v_exp_f32 v57, v57\n\t" \
  "v_smfmac_f32_32x32x32_bf16 v[80:95], v[160:163], v[48:55], v168\n\t" \
  "v_exp_f32 v58, v58\n\t" \
  "v_exp_f32 v59, v59\n\t" \
  "v_exp_f32 v60, v60\n\t" \
  "v_exp_f32 v61, v61\n\t" \
  "v_mfma_f32_32x32x16_bf16 v[16:31], v[140:143], v[134:137], v[16:31]\n\t" \
  "v_exp_f32 v62, v62\n\t" \
  "v_exp_f32 v63, v63\n\t" \
  "s_waitcnt vmcnt(3) lgkmcnt(0)\n\t" \
  "s_barrier\n\t" \
  "s_setprio 3\n\t" \
  "v_exp_f32 v0, v0\n\t" \
  "v_exp_f32 v1, v1\n\t" \
  "v_smfmac_f32_32x32x32_bf16 v[80:95], v[164:167], v[56:63], v168\n\t" \
  "ds_read_b128 v[140:143], v169 offset:0\n\t" \
  "v_exp_f32 v2, v2\n\t" \
  "v_exp_f32 v3, v3\n\t" \
  "v_exp_f32 v4, v4\n\t" \
  "v_exp_f32 v5, v5\n\t" \
  "v_mfma_f32_32x32x8_bf16 v[32:47], v[150:151], v[132:133], 0\n\t" \
  "ds_read_b64 v[144:145], v170 offset:0\n\t" \
  "v_exp_f32 v6, v6\n\t" \
  "v_exp_f32 v7, v7\n\t" \
  "v_exp_f32 v8, v8\n\t" \
  "v_exp_f32 v9, v9\n\t" \
  "v_smfmac_f32_32x32x32_bf16 v[64:79], v[152:155], v[0:7], v168\n\t" \
  "ds_read_b128 v[160:163], v171 offset:34944\n\t" \
  "v_exp_f32 v10, v10\n\t" \
  "v_exp_f32 v11, v11\n\t" \
  "v_exp_f32 v12, v12\n\t" \
  "v_exp_f32 v13, v13\n\t" \
  "v_mfma_f32_32x32x16_bf16 v[32:47], v[146:149], v[128:131], v[32:47]\n\t" \
  "ds_read_b128 v[164:167], v172 offset:34944\n\t" \
  "v_exp_f32 v14, v14\n\t" \
  "v_exp_f32 v15, v15\n\t" \
  "v_exp_f32 v16, v16\n\t" \
  "v_exp_f32 v17, v17\n\t" \
  "v_smfmac_f32_32x32x32_bf16 v[64:79], v[156:159], v[8:15], v168\n\t" \
  "s_setprio 2\n\t" \
  "s_add_u32 m0, %[lds], 21504\n\t" \
  "s_nop 0\n\t" \
  "buffer_load_dwordx4 v173, %[srd], %[so] offen sc1 lds\n\t" \
  "s_mov_b64 exec, 0xfffff\n\t" \
  "buffer_load_dwordx4 v173, %[srd], %[so] offen offset:1024 sc1 lds\n\t" \
  "s_mov_b64 exec, -1\n\t" \
  "s_add_u32 %[so], %[so], 10752\n\t" \
  "v_exp_f32 v18, v18\n\t" \
  "v_exp_f32 v19, v19\n\t" \
  "v_exp_f32 v20, v20\n\t" \
  "v_exp_f32 v21, v21\n\t" \
  "v_mfma_f32_32x32x8_bf16 v[48:63], v[150:151], v[138:139], 0\n\t" \
  "v_exp_f32 v22, v22\n\t" \
  "v_exp_f32 v23, v23\n\t" \
  "v_exp_f32 v24, v24\n\t" \
  "v_exp_f32 v25, v25\n\t" \
  "v_smfmac_f32_32x32x32_bf16 v[80:95], v[152:155], v[16:23], v168\n\t" \
  "v_exp_f32 v26, v26\n\t" \
  "v_exp_f32 v27, v27\n\t" \
  "v_exp_f32 v28, v28\n\t" \
  "v_exp_f32 v29, v29\n\t" \
  "v_mfma_f32_32x32x16_bf16 v[48:63], v[146:149], v[134:137], v[48:63]\n\t" \
  "v_exp_f32 v30, v30\n\t" \
  "v_exp_f32 v31, v31\n\t" \
  "s_waitcnt lgkmcnt(0)\n\t" \
  "s_setprio 1\n\t" \
  "v_exp_f32 v32, v32\n\t" \
  "v_exp_f32 v33, v33\n\t" \
  "v_smfmac_f32_32x32x32_bf16 v[80:95], v[156:159], v[24:31], v168\n\t" \
  "ds_read_b128 v[146:149], v169 offset:2688\n\t" \
  "v_exp_f32 v34, v34\n\t" \
  "v_exp_f32 v35, v35\n\t" \
  "v_exp_f32 v36, v36\n\t" \
  "v_exp_f32 v37, v37\n\t" \
  "v_mfma_f32_32x32x8_bf16 v[0:15], v[144:145], v[132:133], 0\n\t" \
  "ds_read_b64 v[150:151], v170 offset:2688\n\t" \
  "v_exp_f32 v38, v38\n\t" \
  "v_exp_f32 v39, v39\n\t" \
  "v_exp_f32 v40, v40\n\t" \
  "v_exp_f32 v41, v41\n\t" \
  "v_smfmac_f32_32x32x32_bf16 v[64:79], v[160:163], v[32:39], v168\n\t" \
  "ds_read_b128 v[152:155], v171 offset:0\n\t" \
  "v_exp_f32 v42, v42\n\t" \
  "v_exp_f32 v43, v43\n\t" \
  "v_exp_f32 v44, v44\n\t" \
  "v_exp_f32 v45, v45\n\t" \
  "v_mfma_f32_32x32x16_bf16 v[0:15], v[140:143], v[128:131], v[0:15]\n\t" \
  "ds_read_b128 v[156:159], v172 offset:0\n\t" \
  "v_exp_f32 v46, v46\n\t" \
  "v_exp_f32 v47, v47\n\t" \
  "v_exp_f32 v48, v48\n\t" \
  "v_exp_f32 v49, v49\n\t" \
  "v_smfmac_f32_32x32x32_bf16 v[64:79], v[164:167], v[40:47], v168\n\t" \
  "s_setprio 0\n\t" \
  "s_mov_b32 m0, %[wl]\n\t" \
  "s_add_u32 %[wl], %[wl], 1024\n\t" \
  "buffer_load_dwordx4 v96, %[srd], %[ws] offen sc1 lds\n\t" \
  "s_add_u32 %[ws], %[ws], 1024\n\t" \
  "v_exp_f32 v50, v50\n\t" \
  "v_exp_f32 v51, v51\n\t" \
  "v_exp_f32 v52, v52\n\t" \
  "v_exp_f32 v53, v53\n\t" \
  "v_mfma_f32_32x32x8_bf16 v[16:31], v[144:145], v[138:139], 0\n\t" \
  "v_exp_f32 v54, v54\n\t" \
  "v_exp_f32 v55, v55\n\t" \
  "v_exp_f32 v56, v56\n\t" \
  "v_exp_f32 v57, v57\n\t" \
  "v_smfmac_f32_32x32x32_bf16 v[80:95], v[160:163], v[48:55], v168\n\t" \
  "v_exp_f32 v58, v58\n\t" \
  "v_exp_f32 v59, v59\n\t" \
  "v_exp_f32 v60, v60\n\t" \
  "v_exp_f32 v61, v61\n\t" \
  "v_mfma_f32_32x32x16_bf16 v[16:31], v[140:143], v[134:137], v[16:31]\n\t" \
  "s_add_u32 %[trip], %[trip], 1\n\t" \
  "s_cmp_lg_u32 %[trip], 3\n\t" \
  "v_exp_f32 v62, v62\n\t" \
  "v_exp_f32 v63, v63\n\t" \
  "s_cbranch_scc1 Lattn_loop%=\n\t" \
  "s_waitcnt vmcnt(3) lgkmcnt(0)\n\t" \
  "s_barrier\n\t" \
  "s_setprio 3\n\t" \
  "v_exp_f32 v0, v0\n\t" \
  "v_exp_f32 v1, v1\n\t" \
  "v_smfmac_f32_32x32x32_bf16 v[80:95], v[164:167], v[56:63], v168\n\t" \
  "ds_read_b128 v[140:143], v169 offset:10752\n\t" \
  "v_exp_f32 v2, v2\n\t" \
  "v_exp_f32 v3, v3\n\t" \
  "v_exp_f32 v4, v4\n\t" \
  "v_exp_f32 v5, v5\n\t" \
  "v_mfma_f32_32x32x8_bf16 v[32:47], v[150:151], v[132:133], 0\n\t" \
  "ds_read_b64 v[144:145], v170 offset:10752\n\t" \
  "v_exp_f32 v6, v6\n\t" \
  "v_exp_f32 v7, v7\n\t" \
  "v_exp_f32 v8, v8\n\t" \
  "v_exp_f32 v9, v9\n\t" \
  "v_smfmac_f32_32x32x32_bf16 v[64:79], v[152:155], v[0:7], v168\n\t" \
  "ds_read_b128 v[160:163], v171 offset:2688\n\t" \
  "v_exp_f32 v10, v10\n\t" \
  "v_exp_f32 v11, v11\n\t" \
  "v_exp_f32 v12, v12\n\t" \
  "v_exp_f32 v13, v13\n\t" \
  "v_mfma_f32_32x32x16_bf16 v[32:47], v[146:149], v[128:131], v[32:47]\n\t" \
  "ds_read_b128 v[164:167], v172 offset:2688\n\t" \
  "v_exp_f32 v14, v14\n\t" \
  "v_exp_f32 v15, v15\n\t" \
  "v_exp_f32 v16, v16\n\t" \
  "v_exp_f32 v17, v17\n\t" \
  "v_smfmac_f32_32x32x32_bf16 v[64:79], v[156:159], v[8:15], v168\n\t" \
  "s_setprio 2\n\t" \
  "s_add_u32 m0, %[lds], 32256\n\t" \
  "s_nop 0\n\t" \
  "buffer_load_dwordx4 v173, %[srd], %[so] offen sc1 lds\n\t" \
  "s_mov_b64 exec, 0xfffff\n\t" \
  "buffer_load_dwordx4 v173, %[srd], %[so] offen offset:1024 sc1 lds\n\t" \
  "s_mov_b64 exec, -1\n\t" \
  "s_add_u32 %[so], %[so], 10752\n\t" \
  "v_exp_f32 v18, v18\n\t" \
  "v_exp_f32 v19, v19\n\t" \
  "v_exp_f32 v20, v20\n\t" \
  "v_exp_f32 v21, v21\n\t" \
  "v_mfma_f32_32x32x8_bf16 v[48:63], v[150:151], v[138:139], 0\n\t" \
  "v_exp_f32 v22, v22\n\t" \
  "v_exp_f32 v23, v23\n\t" \
  "v_exp_f32 v24, v24\n\t" \
  "v_exp_f32 v25, v25\n\t" \
  "v_smfmac_f32_32x32x32_bf16 v[80:95], v[152:155], v[16:23], v168\n\t" \
  "v_exp_f32 v26, v26\n\t" \
  "v_exp_f32 v27, v27\n\t" \
  "v_exp_f32 v28, v28\n\t" \
  "v_exp_f32 v29, v29\n\t" \
  "v_mfma_f32_32x32x16_bf16 v[48:63], v[146:149], v[134:137], v[48:63]\n\t" \
  "v_exp_f32 v30, v30\n\t" \
  "v_exp_f32 v31, v31\n\t" \
  "s_waitcnt lgkmcnt(0)\n\t" \
  "s_setprio 1\n\t" \
  "v_exp_f32 v32, v32\n\t" \
  "v_exp_f32 v33, v33\n\t" \
  "v_smfmac_f32_32x32x32_bf16 v[80:95], v[156:159], v[24:31], v168\n\t" \
  "ds_read_b128 v[146:149], v169 offset:13440\n\t" \
  "v_exp_f32 v34, v34\n\t" \
  "v_exp_f32 v35, v35\n\t" \
  "v_exp_f32 v36, v36\n\t" \
  "v_exp_f32 v37, v37\n\t" \
  "v_mfma_f32_32x32x8_bf16 v[0:15], v[144:145], v[132:133], 0\n\t" \
  "ds_read_b64 v[150:151], v170 offset:13440\n\t" \
  "v_exp_f32 v38, v38\n\t" \
  "v_exp_f32 v39, v39\n\t" \
  "v_exp_f32 v40, v40\n\t" \
  "v_exp_f32 v41, v41\n\t" \
  "v_smfmac_f32_32x32x32_bf16 v[64:79], v[160:163], v[32:39], v168\n\t" \
  "ds_read_b128 v[152:155], v171 offset:10752\n\t" \
  "v_exp_f32 v42, v42\n\t" \
  "v_exp_f32 v43, v43\n\t" \
  "v_exp_f32 v44, v44\n\t" \
  "v_exp_f32 v45, v45\n\t" \
  "v_mfma_f32_32x32x16_bf16 v[0:15], v[140:143], v[128:131], v[0:15]\n\t" \
  "ds_read_b128 v[156:159], v172 offset:10752\n\t" \
  "v_exp_f32 v46, v46\n\t" \
  "v_exp_f32 v47, v47\n\t" \
  "v_exp_f32 v48, v48\n\t" \
  "v_exp_f32 v49, v49\n\t" \
  "v_smfmac_f32_32x32x32_bf16 v[64:79], v[164:167], v[40:47], v168\n\t" \
  "s_setprio 0\n\t" \
  "s_mov_b32 m0, %[wl]\n\t" \
  "s_add_u32 %[wl], %[wl], 1024\n\t" \
  "buffer_load_dwordx4 v96, %[srd], %[ws] offen sc1 lds\n\t" \
  "s_add_u32 %[ws], %[ws], 1024\n\t" \
  "v_exp_f32 v50, v50\n\t" \
  "v_exp_f32 v51, v51\n\t" \
  "v_exp_f32 v52, v52\n\t" \
  "v_exp_f32 v53, v53\n\t" \
  "v_mfma_f32_32x32x8_bf16 v[16:31], v[144:145], v[138:139], 0\n\t" \
  "v_exp_f32 v54, v54\n\t" \
  "v_exp_f32 v55, v55\n\t" \
  "v_exp_f32 v56, v56\n\t" \
  "v_exp_f32 v57, v57\n\t" \
  "v_smfmac_f32_32x32x32_bf16 v[80:95], v[160:163], v[48:55], v168\n\t" \
  "v_exp_f32 v58, v58\n\t" \
  "v_exp_f32 v59, v59\n\t" \
  "v_exp_f32 v60, v60\n\t" \
  "v_exp_f32 v61, v61\n\t" \
  "v_mfma_f32_32x32x16_bf16 v[16:31], v[140:143], v[134:137], v[16:31]\n\t" \
  "v_exp_f32 v62, v62\n\t" \
  "v_exp_f32 v63, v63\n\t" \
  "s_waitcnt vmcnt(3) lgkmcnt(0)\n\t" \
  "s_barrier\n\t" \
  "s_setprio 3\n\t" \
  "v_exp_f32 v0, v0\n\t" \
  "v_exp_f32 v1, v1\n\t" \
  "v_smfmac_f32_32x32x32_bf16 v[80:95], v[164:167], v[56:63], v168\n\t" \
  "ds_read_b128 v[140:143], v169 offset:21504\n\t" \
  "v_exp_f32 v2, v2\n\t" \
  "v_exp_f32 v3, v3\n\t" \
  "v_exp_f32 v4, v4\n\t" \
  "v_exp_f32 v5, v5\n\t" \
  "v_mfma_f32_32x32x8_bf16 v[32:47], v[150:151], v[132:133], 0\n\t" \
  "ds_read_b64 v[144:145], v170 offset:21504\n\t" \
  "v_exp_f32 v6, v6\n\t" \
  "v_exp_f32 v7, v7\n\t" \
  "v_exp_f32 v8, v8\n\t" \
  "v_exp_f32 v9, v9\n\t" \
  "v_smfmac_f32_32x32x32_bf16 v[64:79], v[152:155], v[0:7], v168\n\t" \
  "ds_read_b128 v[160:163], v171 offset:13440\n\t" \
  "v_exp_f32 v10, v10\n\t" \
  "v_exp_f32 v11, v11\n\t" \
  "v_exp_f32 v12, v12\n\t" \
  "v_exp_f32 v13, v13\n\t" \
  "v_mfma_f32_32x32x16_bf16 v[32:47], v[146:149], v[128:131], v[32:47]\n\t" \
  "ds_read_b128 v[164:167], v172 offset:13440\n\t" \
  "v_exp_f32 v14, v14\n\t" \
  "v_exp_f32 v15, v15\n\t" \
  "v_exp_f32 v16, v16\n\t" \
  "v_exp_f32 v17, v17\n\t" \
  "v_smfmac_f32_32x32x32_bf16 v[64:79], v[156:159], v[8:15], v168\n\t" \
  "s_setprio 2\n\t" \
  "v_exp_f32 v18, v18\n\t" \
  "v_exp_f32 v19, v19\n\t" \
  "v_exp_f32 v20, v20\n\t" \
  "v_exp_f32 v21, v21\n\t" \
  "v_mfma_f32_32x32x8_bf16 v[48:63], v[150:151], v[138:139], 0\n\t" \
  "v_exp_f32 v22, v22\n\t" \
  "v_exp_f32 v23, v23\n\t" \
  "v_exp_f32 v24, v24\n\t" \
  "v_exp_f32 v25, v25\n\t" \
  "v_smfmac_f32_32x32x32_bf16 v[80:95], v[152:155], v[16:23], v168\n\t" \
  "v_exp_f32 v26, v26\n\t" \
  "v_exp_f32 v27, v27\n\t" \
  "v_exp_f32 v28, v28\n\t" \
  "v_exp_f32 v29, v29\n\t" \
  "v_mfma_f32_32x32x16_bf16 v[48:63], v[146:149], v[134:137], v[48:63]\n\t" \
  "v_exp_f32 v30, v30\n\t" \
  "v_exp_f32 v31, v31\n\t" \
  "s_waitcnt lgkmcnt(0)\n\t" \
  "s_setprio 1\n\t" \
  "v_exp_f32 v32, v32\n\t" \
  "v_exp_f32 v33, v33\n\t" \
  "v_smfmac_f32_32x32x32_bf16 v[80:95], v[156:159], v[24:31], v168\n\t" \
  "ds_read_b128 v[146:149], v169 offset:24192\n\t" \
  "v_exp_f32 v34, v34\n\t" \
  "v_exp_f32 v35, v35\n\t" \
  "v_exp_f32 v36, v36\n\t" \
  "v_exp_f32 v37, v37\n\t" \
  "v_mfma_f32_32x32x8_bf16 v[0:15], v[144:145], v[132:133], 0\n\t" \
  "ds_read_b64 v[150:151], v170 offset:24192\n\t" \
  "v_exp_f32 v38, v38\n\t" \
  "v_exp_f32 v39, v39\n\t" \
  "v_exp_f32 v40, v40\n\t" \
  "v_exp_f32 v41, v41\n\t" \
  "v_smfmac_f32_32x32x32_bf16 v[64:79], v[160:163], v[32:39], v168\n\t" \
  "ds_read_b128 v[152:155], v171 offset:21504\n\t" \
  "v_exp_f32 v42, v42\n\t" \
  "v_exp_f32 v43, v43\n\t" \
  "v_exp_f32 v44, v44\n\t" \
  "v_exp_f32 v45, v45\n\t" \
  "v_mfma_f32_32x32x16_bf16 v[0:15], v[140:143], v[128:131], v[0:15]\n\t" \
  "ds_read_b128 v[156:159], v172 offset:21504\n\t" \
  "v_exp_f32 v46, v46\n\t" \
  "v_exp_f32 v47, v47\n\t" \
  "v_exp_f32 v48, v48\n\t" \
  "v_exp_f32 v49, v49\n\t" \
  "v_smfmac_f32_32x32x32_bf16 v[64:79], v[164:167], v[40:47], v168\n\t" \
  "s_setprio 0\n\t" \
  "v_exp_f32 v50, v50\n\t" \
  "v_exp_f32 v51, v51\n\t" \
  "v_exp_f32 v52, v52\n\t" \
  "v_exp_f32 v53, v53\n\t" \
  "v_mfma_f32_32x32x8_bf16 v[16:31], v[144:145], v[138:139], 0\n\t" \
  "v_exp_f32 v54, v54\n\t" \
  "v_exp_f32 v55, v55\n\t" \
  "v_exp_f32 v56, v56\n\t" \
  "v_exp_f32 v57, v57\n\t" \
  "v_smfmac_f32_32x32x32_bf16 v[80:95], v[160:163], v[48:55], v168\n\t" \
  "v_exp_f32 v58, v58\n\t" \
  "v_exp_f32 v59, v59\n\t" \
  "v_exp_f32 v60, v60\n\t" \
  "v_exp_f32 v61, v61\n\t" \
  "v_mfma_f32_32x32x16_bf16 v[16:31], v[140:143], v[134:137], v[16:31]\n\t" \
  "v_exp_f32 v62, v62\n\t" \
  "v_exp_f32 v63, v63\n\t" \
  "s_waitcnt vmcnt(0) lgkmcnt(0)\n\t" \
  "s_barrier\n\t" \
  "s_setprio 3\n\t" \
  "v_exp_f32 v0, v0\n\t" \
  "v_exp_f32 v1, v1\n\t" \
  "v_smfmac_f32_32x32x32_bf16 v[80:95], v[164:167], v[56:63], v168\n\t" \
  "ds_read_b128 v[140:143], v169 offset:32256\n\t" \
  "v_exp_f32 v2, v2\n\t" \
  "v_exp_f32 v3, v3\n\t" \
  "v_exp_f32 v4, v4\n\t" \
  "v_exp_f32 v5, v5\n\t" \
  "v_mfma_f32_32x32x8_bf16 v[32:47], v[150:151], v[132:133], 0\n\t" \
  "ds_read_b64 v[144:145], v170 offset:32256\n\t" \
  "v_exp_f32 v6, v6\n\t" \
  "v_exp_f32 v7, v7\n\t" \
  "v_exp_f32 v8, v8\n\t" \
  "v_exp_f32 v9, v9\n\t" \
  "v_smfmac_f32_32x32x32_bf16 v[64:79], v[152:155], v[0:7], v168\n\t" \
  "ds_read_b128 v[160:163], v171 offset:24192\n\t" \
  "v_exp_f32 v10, v10\n\t" \
  "v_exp_f32 v11, v11\n\t" \
  "v_exp_f32 v12, v12\n\t" \
  "v_exp_f32 v13, v13\n\t" \
  "v_mfma_f32_32x32x16_bf16 v[32:47], v[146:149], v[128:131], v[32:47]\n\t" \
  "ds_read_b128 v[164:167], v172 offset:24192\n\t" \
  "v_exp_f32 v14, v14\n\t" \
  "v_exp_f32 v15, v15\n\t" \
  "v_exp_f32 v16, v16\n\t" \
  "v_exp_f32 v17, v17\n\t" \
  "v_smfmac_f32_32x32x32_bf16 v[64:79], v[156:159], v[8:15], v168\n\t" \
  "s_setprio 2\n\t" \
  "v_exp_f32 v18, v18\n\t" \
  "v_exp_f32 v19, v19\n\t" \
  "v_exp_f32 v20, v20\n\t" \
  "v_exp_f32 v21, v21\n\t" \
  "v_mfma_f32_32x32x8_bf16 v[48:63], v[150:151], v[138:139], 0\n\t" \
  "v_exp_f32 v22, v22\n\t" \
  "v_exp_f32 v23, v23\n\t" \
  "v_exp_f32 v24, v24\n\t" \
  "v_exp_f32 v25, v25\n\t" \
  "v_smfmac_f32_32x32x32_bf16 v[80:95], v[152:155], v[16:23], v168\n\t" \
  "v_exp_f32 v26, v26\n\t" \
  "v_exp_f32 v27, v27\n\t" \
  "v_exp_f32 v28, v28\n\t" \
  "v_exp_f32 v29, v29\n\t" \
  "v_mfma_f32_32x32x16_bf16 v[48:63], v[146:149], v[134:137], v[48:63]\n\t" \
  "v_exp_f32 v30, v30\n\t" \
  "v_exp_f32 v31, v31\n\t" \
  "s_waitcnt lgkmcnt(0)\n\t" \
  "s_setprio 1\n\t" \
  "v_exp_f32 v32, v32\n\t" \
  "v_exp_f32 v33, v33\n\t" \
  "v_smfmac_f32_32x32x32_bf16 v[80:95], v[156:159], v[24:31], v168\n\t" \
  "ds_read_b128 v[146:149], v169 offset:34944\n\t" \
  "v_exp_f32 v34, v34\n\t" \
  "v_exp_f32 v35, v35\n\t" \
  "v_exp_f32 v36, v36\n\t" \
  "v_exp_f32 v37, v37\n\t" \
  "v_mfma_f32_32x32x8_bf16 v[0:15], v[144:145], v[132:133], 0\n\t" \
  "ds_read_b64 v[150:151], v170 offset:34944\n\t" \
  "v_exp_f32 v38, v38\n\t" \
  "v_exp_f32 v39, v39\n\t" \
  "v_exp_f32 v40, v40\n\t" \
  "v_exp_f32 v41, v41\n\t" \
  "v_smfmac_f32_32x32x32_bf16 v[64:79], v[160:163], v[32:39], v168\n\t" \
  "ds_read_b128 v[152:155], v171 offset:32256\n\t" \
  "v_exp_f32 v42, v42\n\t" \
  "v_exp_f32 v43, v43\n\t" \
  "v_exp_f32 v44, v44\n\t" \
  "v_exp_f32 v45, v45\n\t" \
  "v_mfma_f32_32x32x16_bf16 v[0:15], v[140:143], v[128:131], v[0:15]\n\t" \
  "ds_read_b128 v[156:159], v172 offset:32256\n\t" \
  "v_exp_f32 v46, v46\n\t" \
  "v_exp_f32 v47, v47\n\t" \
  "v_exp_f32 v48, v48\n\t" \
  "v_exp_f32 v49, v49\n\t" \
  "v_smfmac_f32_32x32x32_bf16 v[64:79], v[164:167], v[40:47], v168\n\t" \
  "s_setprio 0\n\t" \
  "v_exp_f32 v50, v50\n\t" \
  "v_exp_f32 v51, v51\n\t" \
  "v_exp_f32 v52, v52\n\t" \
  "v_exp_f32 v53, v53\n\t" \
  "v_mfma_f32_32x32x8_bf16 v[16:31], v[144:145], v[138:139], 0\n\t" \
  "v_exp_f32 v54, v54\n\t" \
  "v_exp_f32 v55, v55\n\t" \
  "v_exp_f32 v56, v56\n\t" \
  "v_exp_f32 v57, v57\n\t" \
  "v_smfmac_f32_32x32x32_bf16 v[80:95], v[160:163], v[48:55], v168\n\t" \
  "v_exp_f32 v58, v58\n\t" \
  "v_exp_f32 v59, v59\n\t" \
  "v_exp_f32 v60, v60\n\t" \
  "v_exp_f32 v61, v61\n\t" \
  "v_mfma_f32_32x32x16_bf16 v[16:31], v[140:143], v[134:137], v[16:31]\n\t" \
  "v_exp_f32 v62, v62\n\t" \
  "v_exp_f32 v63, v63\n\t" \
  "s_waitcnt vmcnt(0) lgkmcnt(0)\n\t" \
  "s_setprio 3\n\t" \
  "v_exp_f32 v0, v0\n\t" \
  "v_exp_f32 v1, v1\n\t" \
  "v_smfmac_f32_32x32x32_bf16 v[80:95], v[164:167], v[56:63], v168\n\t" \
  "v_exp_f32 v2, v2\n\t" \
  "v_exp_f32 v3, v3\n\t" \
  "v_exp_f32 v4, v4\n\t" \
  "v_exp_f32 v5, v5\n\t" \
  "v_mfma_f32_32x32x8_bf16 v[32:47], v[150:151], v[132:133], 0\n\t" \
  "v_exp_f32 v6, v6\n\t" \
  "v_exp_f32 v7, v7\n\t" \
  "v_exp_f32 v8, v8\n\t" \
  "v_exp_f32 v9, v9\n\t" \
  "v_smfmac_f32_32x32x32_bf16 v[64:79], v[152:155], v[0:7], v168\n\t" \
  "ds_read_b128 v[160:163], v171 offset:34944\n\t" \
  "v_exp_f32 v10, v10\n\t" \
  "v_exp_f32 v11, v11\n\t" \
  "v_exp_f32 v12, v12\n\t" \
  "v_exp_f32 v13, v13\n\t" \
  "v_mfma_f32_32x32x16_bf16 v[32:47], v[146:149], v[128:131], v[32:47]\n\t" \
  "ds_read_b128 v[164:167], v172 offset:34944\n\t" \
  "v_exp_f32 v14, v14\n\t" \
  "v_exp_f32 v15, v15\n\t" \
  "v_exp_f32 v16, v16\n\t" \
  "v_exp_f32 v17, v17\n\t" \
  "v_smfmac_f32_32x32x32_bf16 v[64:79], v[156:159], v[8:15], v168\n\t" \
  "s_setprio 2\n\t" \
  "v_exp_f32 v18, v18\n\t" \
  "v_exp_f32 v19, v19\n\t" \
  "v_exp_f32 v20, v20\n\t" \
  "v_exp_f32 v21, v21\n\t" \
  "v_mfma_f32_32x32x8_bf16 v[48:63], v[150:151], v[138:139], 0\n\t" \
  "v_exp_f32 v22, v22\n\t" \
  "v_exp_f32 v23, v23\n\t" \
  "v_exp_f32 v24, v24\n\t" \
  "v_exp_f32 v25, v25\n\t" \
  "v_smfmac_f32_32x32x32_bf16 v[80:95], v[152:155], v[16:23], v168\n\t" \
  "v_exp_f32 v26, v26\n\t" \
  "v_exp_f32 v27, v27\n\t" \
  "v_exp_f32 v28, v28\n\t" \
  "v_exp_f32 v29, v29\n\t" \
  "v_mfma_f32_32x32x16_bf16 v[48:63], v[146:149], v[134:137], v[48:63]\n\t" \
  "v_exp_f32 v30, v30\n\t" \
  "v_exp_f32 v31, v31\n\t" \
  "s_waitcnt lgkmcnt(0)\n\t" \
  "s_setprio 1\n\t" \
  "v_exp_f32 v32, v32\n\t" \
  "v_exp_f32 v33, v33\n\t" \
  "v_smfmac_f32_32x32x32_bf16 v[80:95], v[156:159], v[24:31], v168\n\t" \
  "v_exp_f32 v34, v34\n\t" \
  "v_exp_f32 v35, v35\n\t" \
  "v_exp_f32 v36, v36\n\t" \
  "v_exp_f32 v37, v37\n\t" \
  "v_exp_f32 v38, v38\n\t" \
  "v_exp_f32 v39, v39\n\t" \
  "v_exp_f32 v40, v40\n\t" \
  "v_exp_f32 v41, v41\n\t" \
  "v_smfmac_f32_32x32x32_bf16 v[64:79], v[160:163], v[32:39], v168\n\t" \
  "v_exp_f32 v42, v42\n\t" \
  "v_exp_f32 v43, v43\n\t" \
  "v_exp_f32 v44, v44\n\t" \
  "v_exp_f32 v45, v45\n\t" \
  "v_exp_f32 v46, v46\n\t" \
  "v_exp_f32 v47, v47\n\t" \
  "v_exp_f32 v48, v48\n\t" \
  "v_exp_f32 v49, v49\n\t" \
  "v_smfmac_f32_32x32x32_bf16 v[64:79], v[164:167], v[40:47], v168\n\t" \
  "s_setprio 0\n\t" \
  "v_exp_f32 v50, v50\n\t" \
  "v_exp_f32 v51, v51\n\t" \
  "v_exp_f32 v52, v52\n\t" \
  "v_exp_f32 v53, v53\n\t" \
  "v_exp_f32 v54, v54\n\t" \
  "v_exp_f32 v55, v55\n\t" \
  "v_exp_f32 v56, v56\n\t" \
  "v_exp_f32 v57, v57\n\t" \
  "v_smfmac_f32_32x32x32_bf16 v[80:95], v[160:163], v[48:55], v168\n\t" \
  "v_exp_f32 v58, v58\n\t" \
  "v_exp_f32 v59, v59\n\t" \
  "v_exp_f32 v60, v60\n\t" \
  "v_exp_f32 v61, v61\n\t" \
  "v_exp_f32 v62, v62\n\t" \
  "v_exp_f32 v63, v63\n\t" \
  "s_nop 1\n\t" \
  "v_smfmac_f32_32x32x32_bf16 v[80:95], v[164:167], v[56:63], v168\n\t" \
  "s_setprio 0\n\t" \
  "s_nop 10\n\t"
#define ATTN_CLOB "v0", "v1", "v2", "v3", "v4", "v5", "v6", "v7", "v8", "v9", "v10", "v11", "v12", "v13", "v14", "v15", "v16", "v17", "v18", "v19", "v20", "v21", "v22", "v23", "v24", "v25", "v26", "v27", "v28", "v29", "v30", "v31", "v32", "v33", "v34", "v35", "v36", "v37", "v38", "v39", "v40", "v41", "v42", "v43", "v44", "v45", "v46", "v47", "v48", "v49", "v50", "v51", "v52", "v53", "v54", "v55", "v56", "v57", "v58", "v59", "v60", "v61", "v62", "v63", "v105", "v106", "v107", "v108", "v109", "v110", "v128", "v129", "v130", "v131", "v132", "v133", "v134", "v135", "v136", "v137", "v138", "v139", "v140", "v141", "v142", "v143", "v144", "v145", "v146", "v147", "v148", "v149", "v150", "v151", "v152", "v153", "v154", "v155", "v156", "v157", "v158", "v159", "v160", "v161", "v162", "v163", "v164", "v165", "v166", "v167", "v168", "v175"
#define QKV0_ASM \
  "v_mov_b32 v130, v128\n\t" \
  "v_mov_b32 v135, v129\n\t" \
  "s_mov_b32 %[sa], 0\n\t" \
  "s_mov_b32 %[sb], 0\n\t" \
  "s_mov_b32 %[cnt], 9\n\t" \
  "s_barrier\n\t" \
  "ds_read_b128 v[64:67], v130 offset:0\n\t" \
  "ds_read_b128 v[96:99], v135 offset:0\n\t" \
  "ds_read_b128 v[68:71], v130 offset:256\n\t" \
  "ds_read_b128 v[100:103], v135 offset:1024\n\t" \
  "ds_read_b128 v[72:75], v130 offset:512\n\t" \
  "ds_read_b128 v[104:107], v135 offset:2048\n\t" \
  "ds_read_b128 v[76:79], v130 offset:768\n\t" \
  "ds_read_b128 v[108:111], v135 offset:3072\n\t" \
  "LQKV0_loop%=:\n\t" \
  "s_waitcnt lgkmcnt(0)\n\t" \
  "v_mfma_f32_16x16x32_bf16 v[0:3], v[96:99], v[64:67], v[0:3]\n\t" \
  "ds_read_b128 v[80:83], v130 offset:4096\n\t" \
  "v_mfma_f32_16x16x32_bf16 v[4:7], v[100:103], v[64:67], v[4:7]\n\t" \
  "ds_read_b128 v[112:115], v135 offset:8192\n\t" \
  "v_mfma_f32_16x16x32_bf16 v[8:11], v[104:107], v[64:67], v[8:11]\n\t" \
  "ds_read_b128 v[84:87], v130 offset:4352\n\t" \
  "v_mfma_f32_16x16x32_bf16 v[12:15], v[108:111], v[64:67], v[12:15]\n\t" \
  "ds_read_b128 v[116:119], v135 offset:9216\n\t" \
  "v_mfma_f32_16x16x32_bf16 v[28:31], v[108:111], v[68:71], v[28:31]\n\t" \
  "ds_read_b128 v[88:91], v130 offset:4608\n\t" \
  "v_mfma_f32_16x16x32_bf16 v[24:27], v[104:107], v[68:71], v[24:27]\n\t" \
  "ds_read_b128 v[120:123], v135 offset:10240\n\t" \
  "v_mfma_f32_16x16x32_bf16 v[20:23], v[100:103], v[68:71], v[20:23]\n\t" \
  "ds_read_b128 v[92:95], v130 offset:4864\n\t" \
  "v_mfma_f32_16x16x32_bf16 v[16:19], v[96:99], v[68:71], v[16:19]\n\t" \
  "ds_read_b128 v[124:127], v135 offset:11264\n\t" \
  "v_mfma_f32_16x16x32_bf16 v[32:35], v[96:99], v[72:75], v[32:35]\n\t" \
  "s_add_u32 %[sa], %[sa], 16384\n\t" \
  "s_cmp_eq_u32 %[sa], 65536\n\t" \
  "v_mfma_f32_16x16x32_bf16 v[36:39], v[100:103], v[72:75], v[36:39]\n\t" \
  "s_cselect_b32 %[sa], 0, %[sa]\n\t" \
  "v_add_u32 v130, %[sa], v128\n\t" \
  "v_mfma_f32_16x16x32_bf16 v[40:43], v[104:107], v[72:75], v[40:43]\n\t" \
  "s_add_u32 %[sb], %[sb], 16384\n\t" \
  "s_cmp_eq_u32 %[sb], 81920\n\t" \
  "v_mfma_f32_16x16x32_bf16 v[44:47], v[108:111], v[72:75], v[44:47]\n\t" \
  "s_cselect_b32 %[sb], 0, %[sb]\n\t" \
  "v_add_u32 v135, %[sb], v129\n\t" \
  "v_mfma_f32_16x16x32_bf16 v[60:63], v[108:111], v[76:79], v[60:63]\n\t" \
  "v_mfma_f32_16x16x32_bf16 v[56:59], v[104:107], v[76:79], v[56:59]\n\t" \
  "v_mfma_f32_16x16x32_bf16 v[52:55], v[100:103], v[76:79], v[52:55]\n\t" \
  "v_mfma_f32_16x16x32_bf16 v[48:51], v[96:99], v[76:79], v[48:51]\n\t" \
  "s_waitcnt lgkmcnt(0)\n\t" \
  "s_barrier\n\t" \
  "v_mfma_f32_16x16x32_bf16 v[0:3], v[112:115], v[80:83], v[0:3]\n\t" \
  "ds_read_b128 v[64:67], v130 offset:0\n\t" \
  "v_mfma_f32_16x16x32_bf16 v[4:7], v[116:119], v[80:83], v[4:7]\n\t" \
  "ds_read_b128 v[96:99], v135 offset:0\n\t" \
  "v_mfma_f32_16x16x32_bf16 v[8:11], v[120:123], v[80:83], v[8:11]\n\t" \
  "ds_read_b128 v[68:71], v130 offset:256\n\t" \
  "v_mfma_f32_16x16x32_bf16 v[12:15], v[124:127], v[80:83], v[12:15]\n\t" \
  "ds_read_b128 v[100:103], v135 offset:1024\n\t" \
  "v_mfma_f32_16x16x32_bf16 v[28:31], v[124:127], v[84:87], v[28:31]\n\t" \
  "ds_read_b128 v[72:75], v130 offset:512\n\t" \
  "v_mfma_f32_16x16x32_bf16 v[24:27], v[120:123], v[84:87], v[24:27]\n\t" \
  "ds_read_b128 v[104:107], v135 offset:2048\n\t" \
  "v_mfma_f32_16x16x32_bf16 v[20:23], v[116:119], v[84:87], v[20:23]\n\t" \
  "ds_read_b128 v[76:79], v130 offset:768\n\t" \
  "v_mfma_f32_16x16x32_bf16 v[16:19], v[112:115], v[84:87], v[16:19]\n\t" \
  "ds_read_b128 v[108:111], v135 offset:3072\n\t" \
  "v_mfma_f32_16x16x32_bf16 v[32:35], v[112:115], v[88:91], v[32:35]\n\t" \
  "s_sub_u32 %[cnt], %[cnt], 1\n\t" \
  "s_cmp_lg_u32 %[cnt], 0\n\t" \
  "v_mfma_f32_16x16x32_bf16 v[36:39], v[116:119], v[88:91], v[36:39]\n\t" \
  "v_mfma_f32_16x16x32_bf16 v[40:43], v[120:123], v[88:91], v[40:43]\n\t" \
  "v_mfma_f32_16x16x32_bf16 v[44:47], v[124:127], v[88:91], v[44:47]\n\t" \
  "v_mfma_f32_16x16x32_bf16 v[60:63], v[124:127], v[92:95], v[60:63]\n\t" \
  "v_mfma_f32_16x16x32_bf16 v[56:59], v[120:123], v[92:95], v[56:59]\n\t" \
  "v_mfma_f32_16x16x32_bf16 v[52:55], v[116:119], v[92:95], v[52:55]\n\t" \
  "v_mfma_f32_16x16x32_bf16 v[48:51], v[112:115], v[92:95], v[48:51]\n\t" \
  "s_cbranch_scc1 LQKV0_loop%=\n\t" \
  "s_waitcnt lgkmcnt(0)\n\t" \
  "v_mfma_f32_16x16x32_bf16 v[0:3], v[96:99], v[64:67], v[0:3]\n\t" \
  "ds_read_b128 v[80:83], v130 offset:4096\n\t" \
  "v_mfma_f32_16x16x32_bf16 v[4:7], v[100:103], v[64:67], v[4:7]\n\t" \
  "ds_read_b128 v[112:115], v135 offset:8192\n\t" \
  "v_mfma_f32_16x16x32_bf16 v[8:11], v[104:107], v[64:67], v[8:11]\n\t" \
  "ds_read_b128 v[84:87], v130 offset:4352\n\t" \
  "v_mfma_f32_16x16x32_bf16 v[12:15], v[108:111], v[64:67], v[12:15]\n\t" \
  "ds_read_b128 v[116:119], v135 offset:9216\n\t" \
  "v_mfma_f32_16x16x32_bf16 v[28:31], v[108:111], v[68:71], v[28:31]\n\t" \
  "ds_read_b128 v[88:91], v130 offset:4608\n\t" \
  "v_mfma_f32_16x16x32_bf16 v[24:27], v[104:107], v[68:71], v[24:27]\n\t" \
  "ds_read_b128 v[120:123], v135 offset:10240\n\t" \
  "v_mfma_f32_16x16x32_bf16 v[20:23], v[100:103], v[68:71], v[20:23]\n\t" \
  "ds_read_b128 v[92:95], v130 offset:4864\n\t" \
  "v_mfma_f32_16x16x32_bf16 v[16:19], v[96:99], v[68:71], v[16:19]\n\t" \
  "ds_read_b128 v[124:127], v135 offset:11264\n\t" \
  "v_mfma_f32_16x16x32_bf16 v[32:35], v[96:99], v[72:75], v[32:35]\n\t" \
  "v_mfma_f32_16x16x32_bf16 v[36:39], v[100:103], v[72:75], v[36:39]\n\t" \
  "v_mfma_f32_16x16x32_bf16 v[40:43], v[104:107], v[72:75], v[40:43]\n\t" \
  "v_mfma_f32_16x16x32_bf16 v[44:47], v[108:111], v[72:75], v[44:47]\n\t" \
  "v_mfma_f32_16x16x32_bf16 v[60:63], v[108:111], v[76:79], v[60:63]\n\t" \
  "v_mfma_f32_16x16x32_bf16 v[56:59], v[104:107], v[76:79], v[56:59]\n\t" \
  "v_mfma_f32_16x16x32_bf16 v[52:55], v[100:103], v[76:79], v[52:55]\n\t" \
  "v_mfma_f32_16x16x32_bf16 v[48:51], v[96:99], v[76:79], v[48:51]\n\t" \
  "s_waitcnt lgkmcnt(0)\n\t" \
  "s_barrier\n\t" \
  "v_mfma_f32_16x16x32_bf16 v[0:3], v[112:115], v[80:83], v[0:3]\n\t" \
  "ds_read2_b32 v[136:137], v131 offset0:0 offset1:16\n\t" \
  "v_mfma_f32_16x16x32_bf16 v[4:7], v[116:119], v[80:83], v[4:7]\n\t" \
  "ds_read2_b32 v[138:139], v131 offset0:128 offset1:144\n\t" \
  "v_mfma_f32_16x16x32_bf16 v[8:11], v[120:123], v[80:83], v[8:11]\n\t" \
  "ds_read_b128 v[140:143], v132 offset:128\n\t" \
  "v_mfma_f32_16x16x32_bf16 v[12:15], v[124:127], v[80:83], v[12:15]\n\t" \
  "ds_read_b128 v[144:147], v132 offset:192\n\t" \
  "v_mfma_f32_16x16x32_bf16 v[28:31], v[124:127], v[84:87], v[28:31]\n\t" \
  "ds_read_b128 v[148:151], v132 offset:0\n\t" \
  "v_mfma_f32_16x16x32_bf16 v[24:27], v[120:123], v[84:87], v[24:27]\n\t" \
  "ds_read_b128 v[152:155], v132 offset:64\n\t" \
  "v_mfma_f32_16x16x32_bf16 v[20:23], v[116:119], v[84:87], v[20:23]\n\t" \
  "ds_read2_b32 v[156:157], v131 offset0:32 offset1:48\n\t" \
  "v_mfma_f32_16x16x32_bf16 v[16:19], v[112:115], v[84:87], v[16:19]\n\t" \
  "ds_read2_b32 v[158:159], v131 offset0:160 offset1:176\n\t" \
  "v_mfma_f32_16x16x32_bf16 v[32:35], v[112:115], v[88:91], v[32:35]\n\t" \
  "v_mfma_f32_16x16x32_bf16 v[36:39], v[116:119], v[88:91], v[36:39]\n\t" \
  "v_mfma_f32_16x16x32_bf16 v[40:43], v[120:123], v[88:91], v[40:43]\n\t" \
  "v_mfma_f32_16x16x32_bf16 v[44:47], v[124:127], v[88:91], v[44:47]\n\t" \
  "v_mfma_f32_16x16x32_bf16 v[60:63], v[124:127], v[92:95], v[60:63]\n\t" \
  "v_mfma_f32_16x16x32_bf16 v[56:59], v[120:123], v[92:95], v[56:59]\n\t" \
  "v_mfma_f32_16x16x32_bf16 v[52:55], v[116:119], v[92:95], v[52:55]\n\t" \
  "v_mfma_f32_16x16x32_bf16 v[48:51], v[112:115], v[92:95], v[48:51]\n\t" \
  "s_waitcnt lgkmcnt(0)\n\t" \
  "s_nop 6\n\t"
#define QKV0_CLOB "v64", "v65", "v66", "v67", "v68", "v69", "v70", "v71", "v72", "v73", "v74", "v75", "v76", "v77", "v78", "v79", "v80", "v81", "v82", "v83", "v84", "v85", "v86", "v87", "v88", "v89", "v90", "v91", "v92", "v93", "v94", "v95", "v96", "v97", "v98", "v99", "v100", "v101", "v102", "v103", "v104", "v105", "v106", "v107", "v108", "v109", "v110", "v111", "v112", "v113", "v114", "v115", "v116", "v117", "v118", "v119", "v120", "v121", "v122", "v123", "v124", "v125", "v126", "v127", "v130", "v135"
#define QKV0_O0 "={v[136:137]}"
#define QKV0_O1 "={v[138:139]}"
#define QKV0_O2 "={v[140:143]}"
#define QKV0_O3 "={v[144:147]}"
#define QKV0_O4 "={v[148:151]}"
#define QKV0_O5 "={v[152:155]}"
#define QKV0_O6 "={v[156:157]}"
#define QKV0_O7 "={v[158:159]}"
#define QKV0_AA "{v128}"
#define QKV0_AB "{v129}"
#define QKV0_AT0 "{v131}"
#define QKV0_AT1 "{v132}"
#define QKV0_AT2 "{v133}"
#define QKV0_AT3 "{v134}"
#define QKV1_ASM \
  "v_mov_b32 v130, v128\n\t" \
  "v_mov_b32 v135, v129\n\t" \
  "s_mov_b32 %[sa], 0\n\t" \
  "s_mov_b32 %[sb], 0\n\t" \
  "s_mov_b32 %[cnt], 9\n\t" \
  "s_barrier\n\t" \
  "ds_read_b128 v[64:67], v130 offset:0\n\t" \
  "ds_read_b128 v[96:99], v135 offset:0\n\t" \
  "ds_read_b128 v[68:71], v130 offset:256\n\t" \
  "ds_read_b128 v[100:103], v135 offset:1024\n\t" \
  "ds_read_b128 v[72:75], v130 offset:512\n\t" \
  "ds_read_b128 v[104:107], v135 offset:2048\n\t" \
  "ds_read_b128 v[76:79], v130 offset:768\n\t" \
  "ds_read_b128 v[108:111], v135 offset:3072\n\t" \
  "LQKV1_loop%=:\n\t" \
  "s_waitcnt lgkmcnt(0)\n\t" \
  "v_mfma_f32_16x16x32_bf16 v[0:3], v[96:99], v[64:67], v[0:3]\n\t" \
  "ds_read_b128 v[80:83], v130 offset:4096\n\t" \
  "v_mfma_f32_16x16x32_bf16 v[4:7], v[64:67], v[100:103], v[4:7]\n\t" \
  "ds_read_b128 v[112:115], v135 offset:8192\n\t" \
  "v_mfma_f32_16x16x32_bf16 v[8:11], v[64:67], v[104:107], v[8:11]\n\t" \
  "ds_read_b128 v[84:87], v130 offset:4352\n\t" \
  "v_mfma_f32_16x16x32_bf16 v[12:15], v[64:67], v[108:111], v[12:15]\n\t" \
  "ds_read_b128 v[116:119], v135 offset:9216\n\t" \
  "v_mfma_f32_16x16x32_bf16 v[28:31], v[68:71], v[108:111], v[28:31]\n\t" \
  "ds_read_b128 v[88:91], v130 offset:4608\n\t" \
  "v_mfma_f32_16x16x32_bf16 v[24:27], v[68:71], v[104:107], v[24:27]\n\t" \
  "ds_read_b128 v[120:123], v135 offset:10240\n\t" \
  "v_mfma_f32_16x16x32_bf16 v[20:23], v[68:71], v[100:103], v[20:23]\n\t" \
  "ds_read_b128 v[92:95], v130 offset:4864\n\t" \
  "v_mfma_f32_16x16x32_bf16 v[16:19], v[96:99], v[68:71], v[16:19]\n\t" \
  "ds_read_b128 v[124:127], v135 offset:11264\n\t" \
  "v_mfma_f32_16x16x32_bf16 v[32:35], v[96:99], v[72:75], v[32:35]\n\t" \
  "s_add_u32 %[sa], %[sa], 16384\n\t" \
  "s_cmp_eq_u32 %[sa], 65536\n\t" \
  "v_mfma_f32_16x16x32_bf16 v[36:39], v[72:75], v[100:103], v[36:39]\n\t" \
  "s_cselect_b32 %[sa], 0, %[sa]\n\t" \
  "v_add_u32 v130, %[sa], v128\n\t" \
  "v_mfma_f32_16x16x32_bf16 v[40:43], v[72:75], v[104:107], v[40:43]\n\t" \
  "s_add_u32 %[sb], %[sb], 16384\n\t" \
  "s_cmp_eq_u32 %[sb], 81920\n\t" \
  "v_mfma_f32_16x16x32_bf16 v[44:47], v[72:75], v[108:111], v[44:47]\n\t" \
  "s_cselect_b32 %[sb], 0, %[sb]\n\t" \
  "v_add_u32 v135, %[sb], v129\n\t" \
  "v_mfma_f32_16x16x32_bf16 v[60:63], v[76:79], v[108:111], v[60:63]\n\t" \
  "v_mfma_f32_16x16x32_bf16 v[56:59], v[76:79], v[104:107], v[56:59]\n\t" \
  "v_mfma_f32_16x16x32_bf16 v[52:55], v[76:79], v[100:103], v[52:55]\n\t" \
  "v_mfma_f32_16x16x32_bf16 v[48:51], v[96:99], v[76:79], v[48:51]\n\t" \
  "s_waitcnt lgkmcnt(0)\n\t" \
  "s_barrier\n\t" \
  "v_mfma_f32_16x16x32_bf16 v[0:3], v[112:115], v[80:83], v[0:3]\n\t" \
  "ds_read_b128 v[64:67], v130 offset:0\n\t" \
  "v_mfma_f32_16x16x32_bf16 v[4:7], v[80:83], v[116:119], v[4:7]\n\t" \
  "ds_read_b128 v[96:99], v135 offset:0\n\t" \
  "v_mfma_f32_16x16x32_bf16 v[8:11], v[80:83], v[120:123], v[8:11]\n\t" \
  "ds_read_b128 v[68:71], v130 offset:256\n\t" \
  "v_mfma_f32_16x16x32_bf16 v[12:15], v[80:83], v[124:127], v[12:15]\n\t" \
  "ds_read_b128 v[100:103], v135 offset:1024\n\t" \
  "v_mfma_f32_16x16x32_bf16 v[28:31], v[84:87], v[124:127], v[28:31]\n\t" \
  "ds_read_b128 v[72:75], v130 offset:512\n\t" \
  "v_mfma_f32_16x16x32_bf16 v[24:27], v[84:87], v[120:123], v[24:27]\n\t" \
  "ds_read_b128 v[104:107], v135 offset:2048\n\t" \
  "v_mfma_f32_16x16x32_bf16 v[20:23], v[84:87], v[116:119], v[20:23]\n\t" \
  "ds_read_b128 v[76:79], v130 offset:768\n\t" \
  "v_mfma_f32_16x16x32_bf16 v[16:19], v[112:115], v[84:87], v[16:19]\n\t" \
  "ds_read_b128 v[108:111], v135 offset:3072\n\t" \
  "v_mfma_f32_16x16x32_bf16 v[32:35], v[112:115], v[88:91], v[32:35]\n\t" \
  "s_sub_u32 %[cnt], %[cnt], 1\n\t" \
  "s_cmp_lg_u32 %[cnt], 0\n\t" \
  "v_mfma_f32_16x16x32_bf16 v[36:39], v[88:91], v[116:119], v[36:39]\n\t" \
  "v_mfma_f32_16x16x32_bf16 v[40:43], v[88:91], v[120:123], v[40:43]\n\t" \
  "v_mfma_f32_16x16x32_bf16 v[44:47], v[88:91], v[124:127], v[44:47]\n\t" \
  "v_mfma_f32_16x16x32_bf16 v[60:63], v[92:95], v[124:127], v[60:63]\n\t" \
  "v_mfma_f32_16x16x32_bf16 v[56:59], v[92:95], v[120:123], v[56:59]\n\t" \
  "v_mfma_f32_16x16x32_bf16 v[52:55], v[92:95], v[116:119], v[52:55]\n\t" \
  "v_mfma_f32_16x16x32_bf16 v[48:51], v[112:115], v[92:95], v[48:51]\n\t" \
  "s_cbranch_scc1 LQKV1_loop%=\n\t" \
  "s_waitcnt lgkmcnt(0)\n\t" \
  "v_mfma_f32_16x16x32_bf16 v[0:3], v[96:99], v[64:67], v[0:3]\n\t" \
  "ds_read_b128 v[80:83], v130 offset:4096\n\t" \
  "v_mfma_f32_16x16x32_bf16 v[4:7], v[64:67], v[100:103], v[4:7]\n\t" \
  "ds_read_b128 v[112:115], v135 offset:8192\n\t" \
  "v_mfma_f32_16x16x32_bf16 v[8:11], v[64:67], v[104:107], v[8:11]\n\t" \
  "ds_read_b128 v[84:87], v130 offset:4352\n\t" \
  "v_mfma_f32_16x16x32_bf16 v[12:15], v[64:67], v[108:111], v[12:15]\n\t" \
  "ds_read_b128 v[116:119], v135 offset:9216\n\t" \
  "v_mfma_f32_16x16x32_bf16 v[28:31], v[68:71], v[108:111], v[28:31]\n\t" \
  "ds_read_b128 v[88:91], v130 offset:4608\n\t" \
  "v_mfma_f32_16x16x32_bf16 v[24:27], v[68:71], v[104:107], v[24:27]\n\t" \
  "ds_read_b128 v[120:123], v135 offset:10240\n\t" \
  "v_mfma_f32_16x16x32_bf16 v[20:23], v[68:71], v[100:103], v[20:23]\n\t" \
  "ds_read_b128 v[92:95], v130 offset:4864\n\t" \
  "v_mfma_f32_16x16x32_bf16 v[16:19], v[96:99], v[68:71], v[16:19]\n\t" \
  "ds_read_b128 v[124:127], v135 offset:11264\n\t" \
  "v_mfma_f32_16x16x32_bf16 v[32:35], v[96:99], v[72:75], v[32:35]\n\t" \
  "v_mfma_f32_16x16x32_bf16 v[36:39], v[72:75], v[100:103], v[36:39]\n\t" \
  "v_mfma_f32_16x16x32_bf16 v[40:43], v[72:75], v[104:107], v[40:43]\n\t" \
  "v_mfma_f32_16x16x32_bf16 v[44:47], v[72:75], v[108:111], v[44:47]\n\t" \
  "v_mfma_f32_16x16x32_bf16 v[60:63], v[76:79], v[108:111], v[60:63]\n\t" \
  "v_mfma_f32_16x16x32_bf16 v[56:59], v[76:79], v[104:107], v[56:59]\n\t" \
  "v_mfma_f32_16x16x32_bf16 v[52:55], v[76:79], v[100:103], v[52:55]\n\t" \
  "v_mfma_f32_16x16x32_bf16 v[48:51], v[96:99], v[76:79], v[48:51]\n\t" \
  "s_waitcnt lgkmcnt(0)\n\t" \
  "s_barrier\n\t" \
  "v_mfma_f32_16x16x32_bf16 v[0:3], v[112:115], v[80:83], v[0:3]\n\t" \
  "ds_read_b128 v[136:139], v133 offset:0\n\t" \
  "v_mfma_f32_16x16x32_bf16 v[4:7], v[80:83], v[116:119], v[4:7]\n\t" \
  "ds_read_b128 v[140:143], v133 offset:64\n\t" \
  "v_mfma_f32_16x16x32_bf16 v[8:11], v[80:83], v[120:123], v[8:11]\n\t" \
  "ds_read_b128 v[144:147], v133 offset:512\n\t" \
  "v_mfma_f32_16x16x32_bf16 v[12:15], v[80:83], v[124:127], v[12:15]\n\t" \
  "ds_read_b128 v[148:151], v133 offset:576\n\t" \
  "v_mfma_f32_16x16x32_bf16 v[28:31], v[84:87], v[124:127], v[28:31]\n\t" \
  "ds_read2_b32 v[152:153], v134 offset0:0 offset1:16\n\t" \
  "v_mfma_f32_16x16x32_bf16 v[24:27], v[84:87], v[120:123], v[24:27]\n\t" \
  "ds_read_b32 v154, v134 offset:128\n\t" \
  "v_mfma_f32_16x16x32_bf16 v[20:23], v[84:87], v[116:119], v[20:23]\n\t" \
  "ds_read_b128 v[156:159], v132 offset:256\n\t" \
  "v_mfma_f32_16x16x32_bf16 v[16:19], v[112:115], v[84:87], v[16:19]\n\t" \
  "ds_read2_b32 v[160:161], v131 offset0:0 offset1:16\n\t" \
  "v_mfma_f32_16x16x32_bf16 v[32:35], v[112:115], v[88:91], v[32:35]\n\t" \
  "ds_read2_b32 v[162:163], v131 offset0:128 offset1:144\n\t" \
  "v_mfma_f32_16x16x32_bf16 v[36:39], v[88:91], v[116:119], v[36:39]\n\t" \
  "ds_read_b128 v[164:167], v133 offset:128\n\t" \
  "v_mfma_f32_16x16x32_bf16 v[40:43], v[88:91], v[120:123], v[40:43]\n\t" \
  "ds_read_b128 v[168:171], v133 offset:192\n\t" \
  "v_mfma_f32_16x16x32_bf16 v[44:47], v[88:91], v[124:127], v[44:47]\n\t" \
  "ds_read_b128 v[172:175], v133 offset:640\n\t" \
  "v_mfma_f32_16x16x32_bf16 v[60:63], v[92:95], v[124:127], v[60:63]\n\t" \
  "ds_read_b128 v[176:179], v133 offset:704\n\t" \
  "v_mfma_f32_16x16x32_bf16 v[56:59], v[92:95], v[120:123], v[56:59]\n\t" \
  "ds_read2_b32 v[180:181], v131 offset0:32 offset1:48\n\t" \
  "v_mfma_f32_16x16x32_bf16 v[52:55], v[92:95], v[116:119], v[52:55]\n\t" \
  "ds_read2_b32 v[182:183], v131 offset0:160 offset1:176\n\t" \
  "v_mfma_f32_16x16x32_bf16 v[48:51], v[112:115], v[92:95], v[48:51]\n\t" \
  "s_waitcnt lgkmcnt(0)\n\t" \
  "s_nop 6\n\t"
#define QKV1_CLOB "v64", "v65", "v66", "v67", "v68", "v69", "v70", "v71", "v72", "v73", "v74", "v75", "v76", "v77", "v78", "v79", "v80", "v81", "v82", "v83", "v84", "v85", "v86", "v87", "v88", "v89", "v90", "v91", "v92", "v93", "v94", "v95", "v96", "v97", "v98", "v99", "v100", "v101", "v102", "v103", "v104", "v105", "v106", "v107", "v108", "v109", "v110", "v111", "v112", "v113", "v114", "v115", "v116", "v117", "v118", "v119", "v120", "v121", "v122", "v123", "v124", "v125", "v126", "v127", "v130", "v135"
#define QKV1_O0 "={v[136:139]}"
#define QKV1_O1 "={v[140:143]}"
#define QKV1_O2 "={v[144:147]}"
#define QKV1_O3 "={v[148:151]}"
#define QKV1_O4 "={v[152:153]}"
#define QKV1_O5 "={v154}"
#define QKV1_O6 "={v[156:159]}"
#define QKV1_O7 "={v[160:161]}"
#define QKV1_O8 "={v[162:163]}"
#define QKV1_O9 "={v[164:167]}"
#define QKV1_O10 "={v[168:171]}"
#define QKV1_O11 "={v[172:175]}"
#define QKV1_O12 "={v[176:179]}"
#define QKV1_O13 "={v[180:181]}"
#define QKV1_O14 "={v[182:183]}"
#define QKV1_AA "{v128}"
#define QKV1_AB "{v129}"
#define QKV1_AT0 "{v131}"
#define QKV1_AT1 "{v132}"
#define QKV1_AT2 "{v133}"
#define QKV1_AT3 "{v134}"
#define GU_ASM \
  "v_mov_b32 v178, v176\n\t" \
  "v_mov_b32 v183, v177\n\t" \
  "s_mov_b32 %[sa], 0\n\t" \
  "s_mov_b32 %[sb], 0\n\t" \
  "s_mov_b32 %[cnt], 9\n\t" \
  "s_barrier\n\t" \
  "ds_read_b128 v[96:99], v178 offset:0\n\t" \
  "ds_read_b128 v[128:131], v183 offset:0\n\t" \
  "ds_read_b128 v[100:103], v178 offset:256\n\t" \
  "ds_read_b128 v[132:135], v183 offset:1024\n\t" \
  "ds_read_b128 v[104:107], v178 offset:512\n\t" \
  "ds_read_b128 v[136:139], v183 offset:2048\n\t" \
  "ds_read_b128 v[108:111], v178 offset:768\n\t" \
  "ds_read_b128 v[140:143], v183 offset:3072\n\t" \
  "ds_read_b128 v[144:147], v183 offset:4096\n\t" \
  "ds_read_b128 v[148:151], v183 offset:5120\n\t" \
  "LGU_loop%=:\n\t" \
  "s_waitcnt lgkmcnt(0)\n\t" \
  "v_mfma_f32_16x16x32_bf16 v[0:3], v[128:131], v[96:99], v[0:3]\n\t" \
  "ds_read_b128 v[112:115], v178 offset:4096\n\t" \
  "v_mfma_f32_16x16x32_bf16 v[4:7], v[132:135], v[96:99], v[4:7]\n\t" \
  "ds_read_b128 v[152:155], v183 offset:12288\n\t" \
  "v_mfma_f32_16x16x32_bf16 v[8:11], v[136:139], v[96:99], v[8:11]\n\t" \
  "ds_read_b128 v[116:119], v178 offset:4352\n\t" \
  "v_mfma_f32_16x16x32_bf16 v[12:15], v[140:143], v[96:99], v[12:15]\n\t" \
  "ds_read_b128 v[156:159], v183 offset:13312\n\t" \
  "v_mfma_f32_16x16x32_bf16 v[16:19], v[144:147], v[96:99], v[16:19]\n\t" \
  "ds_read_b128 v[120:123], v178 offset:4608\n\t" \
  "v_mfma_f32_16x16x32_bf16 v[20:23], v[148:151], v[96:99], v[20:23]\n\t" \
  "ds_read_b128 v[160:163], v183 offset:14336\n\t" \
  "v_mfma_f32_16x16x32_bf16 v[44:47], v[148:151], v[100:103], v[44:47]\n\t" \
  "ds_read_b128 v[124:127], v178 offset:4864\n\t" \
  "v_mfma_f32_16x16x32_bf16 v[40:43], v[144:147], v[100:103], v[40:43]\n\t" \
  "ds_read_b128 v[164:167], v183 offset:15360\n\t" \
  "v_mfma_f32_16x16x32_bf16 v[36:39], v[140:143], v[100:103], v[36:39]\n\t" \
  "ds_read_b128 v[168:171], v183 offset:16384\n\t" \
  "v_mfma_f32_16x16x32_bf16 v[32:35], v[136:139], v[100:103], v[32:35]\n\t" \
  "ds_read_b128 v[172:175], v183 offset:17408\n\t" \
  "v_mfma_f32_16x16x32_bf16 v[28:31], v[132:135], v[100:103], v[28:31]\n\t" \
  "s_add_u32 %[sa], %[sa], 16384\n\t" \
  "s_cmp_eq_u32 %[sa], 49152\n\t" \
  "v_mfma_f32_16x16x32_bf16 v[24:27], v[128:131], v[100:103], v[24:27]\n\t" \
  "s_cselect_b32 %[sa], 0, %[sa]\n\t" \
  "v_add_u32 v178, %[sa], v176\n\t" \
  "v_mfma_f32_16x16x32_bf16 v[48:51], v[128:131], v[104:107], v[48:51]\n\t" \
  "s_add_u32 %[sb], %[sb], 24576\n\t" \
  "s_cmp_eq_u32 %[sb], 98304\n\t" \
  "v_mfma_f32_16x16x32_bf16 v[52:55], v[132:135], v[104:107], v[52:55]\n\t" \
  "s_cselect_b32 %[sb], 0, %[sb]\n\t" \
  "v_add_u32 v183, %[sb], v177\n\t" \
  "v_mfma_f32_16x16x32_bf16 v[56:59], v[136:139], v[104:107], v[56:59]\n\t" \
  "v_mfma_f32_16x16x32_bf16 v[60:63], v[140:143], v[104:107], v[60:63]\n\t" \
  "v_mfma_f32_16x16x32_bf16 v[64:67], v[144:147], v[104:107], v[64:67]\n\t" \
  "v_mfma_f32_16x16x32_bf16 v[68:71], v[148:151], v[104:107], v[68:71]\n\t" \
  "v_mfma_f32_16x16x32_bf16 v[92:95], v[148:151], v[108:111], v[92:95]\n\t" \
  "v_mfma_f32_16x16x32_bf16 v[88:91], v[144:147], v[108:111], v[88:91]\n\t" \
  "v_mfma_f32_16x16x32_bf16 v[84:87], v[140:143], v[108:111], v[84:87]\n\t" \
  "v_mfma_f32_16x16x32_bf16 v[80:83], v[136:139], v[108:111], v[80:83]\n\t" \
  "v_mfma_f32_16x16x32_bf16 v[76:79], v[132:135], v[108:111], v[76:79]\n\t" \
  "v_mfma_f32_16x16x32_bf16 v[72:75], v[128:131], v[108:111], v[72:75]\n\t" \
  "s_waitcnt lgkmcnt(0)\n\t" \
  "s_barrier\n\t" \
  "v_mfma_f32_16x16x32_bf16 v[0:3], v[152:155], v[112:115], v[0:3]\n\t" \
  "ds_read_b128 v[96:99], v178 offset:0\n\t" \
  "v_mfma_f32_16x16x32_bf16 v[4:7], v[156:159], v[112:115], v[4:7]\n\t" \
  "ds_read_b128 v[128:131], v183 offset:0\n\t" \
  "v_mfma_f32_16x16x32_bf16 v[8:11], v[160:163], v[112:115], v[8:11]\n\t" \
  "ds_read_b128 v[100:103], v178 offset:256\n\t" \
  "v_mfma_f32_16x16x32_bf16 v[12:15], v[164:167], v[112:115], v[12:15]\n\t" \
  "ds_read_b128 v[132:135], v183 offset:1024\n\t" \
  "v_mfma_f32_16x16x32_bf16 v[16:19], v[168:171], v[112:115], v[16:19]\n\t" \
  "ds_read_b128 v[104:107], v178 offset:512\n\t" \
  "v_mfma_f32_16x16x32_bf16 v[20:23], v[172:175], v[112:115], v[20:23]\n\t" \
  "ds_read_b128 v[136:139], v183 offset:2048\n\t" \
  "v_mfma_f32_16x16x32_bf16 v[44:47], v[172:175], v[116:119], v[44:47]\n\t" \
  "ds_read_b128 v[108:111], v178 offset:768\n\t" \
  "v_mfma_f32_16x16x32_bf16 v[40:43], v[168:171], v[116:119], v[40:43]\n\t" \
  "ds_read_b128 v[140:143], v183 offset:3072\n\t" \
  "v_mfma_f32_16x16x32_bf16 v[36:39], v[164:167], v[116:119], v[36:39]\n\t" \
  "ds_read_b128 v[144:147], v183 offset:4096\n\t" \
  "v_mfma_f32_16x16x32_bf16 v[32:35], v[160:163], v[116:119], v[32:35]\n\t" \
  "ds_read_b128 v[148:151], v183 offset:5120\n\t" \
  "v_mfma_f32_16x16x32_bf16 v[28:31], v[156:159], v[116:119], v[28:31]\n\t" \
  "s_sub_u32 %[cnt], %[cnt], 1\n\t" \
  "s_cmp_lg_u32 %[cnt], 0\n\t" \
  "v_mfma_f32_16x16x32_bf16 v[24:27], v[152:155], v[116:119], v[24:27]\n\t" \
  "v_mfma_f32_16x16x32_bf16 v[48:51], v[152:155], v[120:123], v[48:51]\n\t" \
  "v_mfma_f32_16x16x32_bf16 v[52:55], v[156:159], v[120:123], v[52:55]\n\t" \
  "v_mfma_f32_16x16x32_bf16 v[56:59], v[160:163], v[120:123], v[56:59]\n\t" \
  "v_mfma_f32_16x16x32_bf16 v[60:63], v[164:167], v[120:123], v[60:63]\n\t" \
  "v_mfma_f32_16x16x32_bf16 v[64:67], v[168:171], v[120:123], v[64:67]\n\t" \
  "v_mfma_f32_16x16x32_bf16 v[68:71], v[172:175], v[120:123], v[68:71]\n\t" \
  "v_mfma_f32_16x16x32_bf16 v[92:95], v[172:175], v[124:127], v[92:95]\n\t" \
  "v_mfma_f32_16x16x32_bf16 v[88:91], v[168:171], v[124:127], v[88:91]\n\t" \
  "v_mfma_f32_16x16x32_bf16 v[84:87], v[164:167], v[124:127], v[84:87]\n\t" \
  "v_mfma_f32_16x16x32_bf16 v[80:83], v[160:163], v[124:127], v[80:83]\n\t" \
  "v_mfma_f32_16x16x32_bf16 v[76:79], v[156:159], v[124:127], v[76:79]\n\t" \
  "v_mfma_f32_16x16x32_bf16 v[72:75], v[152:155], v[124:127], v[72:75]\n\t" \
  "s_cbranch_scc1 LGU_loop%=\n\t" \
  "s_waitcnt lgkmcnt(0)\n\t" \
  "v_mfma_f32_16x16x32_bf16 v[0:3], v[128:131], v[96:99], v[0:3]\n\t" \
  "ds_read_b128 v[112:115], v178 offset:4096\n\t" \
  "v_mfma_f32_16x16x32_bf16 v[4:7], v[132:135], v[96:99], v[4:7]\n\t" \
  "ds_read_b128 v[152:155], v183 offset:12288\n\t" \
  "v_mfma_f32_16x16x32_bf16 v[8:11], v[136:139], v[96:99], v[8:11]\n\t" \
  "ds_read_b128 v[116:119], v178 offset:4352\n\t" \
  "v_mfma_f32_16x16x32_bf16 v[12:15], v[140:143], v[96:99], v[12:15]\n\t" \
  "ds_read_b128 v[156:159], v183 offset:13312\n\t" \
  "v_mfma_f32_16x16x32_bf16 v[16:19], v[144:147], v[96:99], v[16:19]\n\t" \
  "ds_read_b128 v[120:123], v178 offset:4608\n\t" \
  "v_mfma_f32_16x16x32_bf16 v[20:23], v[148:151], v[96:99], v[20:23]\n\t" \
  "ds_read_b128 v[160:163], v183 offset:14336\n\t" \
  "v_mfma_f32_16x16x32_bf16 v[44:47], v[148:151], v[100:103], v[44:47]\n\t" \
  "ds_read_b128 v[124:127], v178 offset:4864\n\t" \
  "v_mfma_f32_16x16x32_bf16 v[40:43], v[144:147], v[100:103], v[40:43]\n\t" \
  "ds_read_b128 v[164:167], v183 offset:15360\n\t" \
  "v_mfma_f32_16x16x32_bf16 v[36:39], v[140:143], v[100:103], v[36:39]\n\t" \
  "ds_read_b128 v[168:171], v183 offset:16384\n\t" \
  "v_mfma_f32_16x16x32_bf16 v[32:35], v[136:139], v[100:103], v[32:35]\n\t" \
  "ds_read_b128 v[172:175], v183 offset:17408\n\t" \
  "v_mfma_f32_16x16x32_bf16 v[28:31], v[132:135], v[100:103], v[28:31]\n\t" \
  "v_mfma_f32_16x16x32_bf16 v[24:27], v[128:131], v[100:103], v[24:27]\n\t" \
  "v_mfma_f32_16x16x32_bf16 v[48:51], v[128:131], v[104:107], v[48:51]\n\t" \
  "v_mfma_f32_16x16x32_bf16 v[52:55], v[132:135], v[104:107], v[52:55]\n\t" \
  "v_mfma_f32_16x16x32_bf16 v[56:59], v[136:139], v[104:107], v[56:59]\n\t" \
  "v_mfma_f32_16x16x32_bf16 v[60:63], v[140:143], v[104:107], v[60:63]\n\t" \
  "v_mfma_f32_16x16x32_bf16 v[64:67], v[144:147], v[104:107], v[64:67]\n\t" \
  "v_mfma_f32_16x16x32_bf16 v[68:71], v[148:151], v[104:107], v[68:71]\n\t" \
  "v_mfma_f32_16x16x32_bf16 v[92:95], v[148:151], v[108:111], v[92:95]\n\t" \
  "v_mfma_f32_16x16x32_bf16 v[88:91], v[144:147], v[108:111], v[88:91]\n\t" \
  "v_mfma_f32_16x16x32_bf16 v[84:87], v[140:143], v[108:111], v[84:87]\n\t" \
  "v_mfma_f32_16x16x32_bf16 v[80:83], v[136:139], v[108:111], v[80:83]\n\t" \
  "v_mfma_f32_16x16x32_bf16 v[76:79], v[132:135], v[108:111], v[76:79]\n\t" \
  "v_mfma_f32_16x16x32_bf16 v[72:75], v[128:131], v[108:111], v[72:75]\n\t" \
  "s_waitcnt lgkmcnt(0)\n\t" \
  "s_barrier\n\t" \
  "v_mfma_f32_16x16x32_bf16 v[0:3], v[152:155], v[112:115], v[0:3]\n\t" \
  "ds_read2_b32 v[184:185], v179 offset0:0 offset1:16\n\t" \
  "v_mfma_f32_16x16x32_bf16 v[4:7], v[156:159], v[112:115], v[4:7]\n\t" \
  "ds_read2_b32 v[186:187], v179 offset0:128 offset1:144\n\t" \
  "v_mfma_f32_16x16x32_bf16 v[8:11], v[160:163], v[112:115], v[8:11]\n\t" \
  "ds_read_b128 v[188:191], v180 offset:0\n\t" \
  "v_mfma_f32_16x16x32_bf16 v[12:15], v[164:167], v[112:115], v[12:15]\n\t" \
  "ds_read_b128 v[192:195], v180 offset:64\n\t" \
  "v_mfma_f32_16x16x32_bf16 v[16:19], v[168:171], v[112:115], v[16:19]\n\t" \
  "ds_read_b128 v[196:199], v180 offset:128\n\t" \
  "v_mfma_f32_16x16x32_bf16 v[20:23], v[172:175], v[112:115], v[20:23]\n\t" \
  "ds_read_b128 v[200:203], v180 offset:192\n\t" \
  "v_mfma_f32_16x16x32_bf16 v[44:47], v[172:175], v[116:119], v[44:47]\n\t" \
  "ds_read_b128 v[204:207], v180 offset:256\n\t" \
  "v_mfma_f32_16x16x32_bf16 v[40:43], v[168:171], v[116:119], v[40:43]\n\t" \
  "ds_read_b128 v[208:211], v180 offset:320\n\t" \
  "v_mfma_f32_16x16x32_bf16 v[36:39], v[164:167], v[116:119], v[36:39]\n\t" \
  "ds_read2_b32 v[212:213], v179 offset0:32 offset1:48\n\t" \
  "v_mfma_f32_16x16x32_bf16 v[32:35], v[160:163], v[116:119], v[32:35]\n\t" \
  "ds_read2_b32 v[214:215], v179 offset0:160 offset1:176\n\t" \
  "v_mfma_f32_16x16x32_bf16 v[28:31], v[156:159], v[116:119], v[28:31]\n\t" \
  "v_mfma_f32_16x16x32_bf16 v[24:27], v[152:155], v[116:119], v[24:27]\n\t" \
  "v_mfma_f32_16x16x32_bf16 v[48:51], v[152:155], v[120:123], v[48:51]\n\t" \
  "v_mfma_f32_16x16x32_bf16 v[52:55], v[156:159], v[120:123], v[52:55]\n\t" \
  "v_mfma_f32_16x16x32_bf16 v[56:59], v[160:163], v[120:123], v[56:59]\n\t" \
  "v_mfma_f32_16x16x32_bf16 v[60:63], v[164:167], v[120:123], v[60:63]\n\t" \
  "v_mfma_f32_16x16x32_bf16 v[64:67], v[168:171], v[120:123], v[64:67]\n\t" \
  "v_mfma_f32_16x16x32_bf16 v[68:71], v[172:175], v[120:123], v[68:71]\n\t" \
  "v_mfma_f32_16x16x32_bf16 v[92:95], v[172:175], v[124:127], v[92:95]\n\t" \
  "v_mfma_f32_16x16x32_bf16 v[88:91], v[168:171], v[124:127], v[88:91]\n\t" \
  "v_mfma_f32_16x16x32_bf16 v[84:87], v[164:167], v[124:127], v[84:87]\n\t" \
  "v_mfma_f32_16x16x32_bf16 v[80:83], v[160:163], v[124:127], v[80:83]\n\t" \
  "v_mfma_f32_16x16x32_bf16 v[76:79], v[156:159], v[124:127], v[76:79]\n\t" \
  "v_mfma_f32_16x16x32_bf16 v[72:75], v[152:155], v[124:127], v[72:75]\n\t" \
  "s_waitcnt lgkmcnt(0)\n\t" \
  "s_nop 6\n\t"
#define GU_CLOB "v96", "v97", "v98", "v99", "v100", "v101", "v102", "v103", "v104", "v105", "v106", "v107", "v108", "v109", "v110", "v111", "v112", "v113", "v114", "v115", "v116", "v117", "v118", "v119", "v120", "v121", "v122", "v123", "v124", "v125", "v126", "v127", "v128", "v129", "v130", "v131", "v132", "v133", "v134", "v135", "v136", "v137", "v138", "v139", "v140", "v141", "v142", "v143", "v144", "v145", "v146", "v147", "v148", "v149", "v150", "v151", "v152", "v153", "v154", "v155", "v156", "v157", "v158", "v159", "v160", "v161", "v162", "v163", "v164", "v165", "v166", "v167", "v168", "v169", "v170", "v171", "v172", "v173", "v174", "v175", "v178", "v183"
#define GU_O0 "={v[184:185]}"
#define GU_O1 "={v[186:187]}"
#define GU_O2 "={v[188:191]}"
#define GU_O3 "={v[192:195]}"
#define GU_O4 "={v[196:199]}"
#define GU_O5 "={v[200:203]}"
#define GU_O6 "={v[204:207]}"
#define GU_O7 "={v[208:211]}"
#define GU_O8 "={v[212:213]}"
#define GU_O9 "={v[214:215]}"
#define GU_AA "{v176}"
#define GU_AB "{v177}"
#define GU_AT0 "{v179}"
#define GU_AT1 "{v180}"
#define GU_AT2 "{v181}"
#define GU_AT3 "{v182}"
#define WO_ASM \
  "v_add_u32 v147, 61440, v146\n\t" \
  "v_add_u32 v148, 122880, v146\n\t" \
  "s_barrier\n\t" \
  "buffer_load_dwordx4 v[80:83], v144, %[srd], %[so] offen sc1\n\t" \
  "buffer_load_dwordx4 v[84:87], v145, %[srd], %[so] offen sc1\n\t" \
  "s_add_u32 %[so], %[so], 8192\n\t" \
  "buffer_load_dwordx4 v[88:91], v144, %[srd], %[so] offen sc1\n\t" \
  "buffer_load_dwordx4 v[92:95], v145, %[srd], %[so] offen sc1\n\t" \
  "s_add_u32 %[so], %[so], 8192\n\t" \
  "buffer_load_dwordx4 v[96:99], v144, %[srd], %[so] offen sc1\n\t" \
  "buffer_load_dwordx4 v[100:103], v145, %[srd], %[so] offen sc1\n\t" \
  "s_add_u32 %[so], %[so], 8192\n\t" \
  "buffer_load_dwordx4 v[104:107], v144, %[srd], %[so] offen sc1\n\t" \
  "buffer_load_dwordx4 v[108:111], v145, %[srd], %[so] offen sc1\n\t" \
  "s_add_u32 %[so], %[so], 8192\n\t" \
  "buffer_load_dwordx4 v[112:115], v144, %[srd], %[so] offen sc1\n\t" \
  "buffer_load_dwordx4 v[116:119], v145, %[srd], %[so] offen sc1\n\t" \
  "s_add_u32 %[so], %[so], 8192\n\t" \
  "ds_read_b128 v[40:43], v146 offset:58368\n\t" \
  "ds_read_b128 v[44:47], v146 offset:59392\n\t" \
  "ds_read_b128 v[48:51], v146 offset:60416\n\t" \
  "ds_read_b128 v[52:55], v146 offset:61440\n\t" \
  "ds_read_b128 v[56:59], v146 offset:62464\n\t" \
  "s_waitcnt vmcnt(8) lgkmcnt(0)\n\t" \
  "v_mfma_f32_16x16x32_bf16 v[0:3], v[40:43], v[80:83], v[0:3]\n\t" \
  "ds_read_b128 v[60:63], v147 offset:7168\n\t" \
  "v_mfma_f32_16x16x32_bf16 v[4:7], v[44:47], v[80:83], v[4:7]\n\t" \
  "ds_read_b128 v[64:67], v147 offset:8192\n\t" \
  "v_mfma_f32_16x16x32_bf16 v[8:11], v[48:51], v[80:83], v[8:11]\n\t" \
  "ds_read_b128 v[68:71], v147 offset:9216\n\t" \
  "v_mfma_f32_16x16x32_bf16 v[12:15], v[52:55], v[80:83], v[12:15]\n\t" \
  "ds_read_b128 v[72:75], v147 offset:10240\n\t" \
  "v_mfma_f32_16x16x32_bf16 v[16:19], v[56:59], v[80:83], v[16:19]\n\t" \
  "ds_read_b128 v[76:79], v147 offset:11264\n\t" \
  "v_mfma_f32_16x16x32_bf16 v[20:23], v[40:43], v[84:87], v[20:23]\n\t" \
  "buffer_load_dwordx4 v[120:123], v144, %[srd], %[so] offen sc1\n\t" \
  "v_mfma_f32_16x16x32_bf16 v[24:27], v[44:47], v[84:87], v[24:27]\n\t" \
  "buffer_load_dwordx4 v[124:127], v145, %[srd], %[so] offen sc1\n\t" \
  "s_add_u32 %[so], %[so], 8192\n\t" \
  "v_mfma_f32_16x16x32_bf16 v[28:31], v[48:51], v[84:87], v[28:31]\n\t" \
  "buffer_load_dwordx4 v[128:131], v144, %[srd], %[so] offen sc1\n\t" \
  "v_mfma_f32_16x16x32_bf16 v[32:35], v[52:55], v[84:87], v[32:35]\n\t" \
  "buffer_load_dwordx4 v[132:135], v145, %[srd], %[so] offen sc1\n\t" \
  "s_add_u32 %[so], %[so], 8192\n\t" \
  "v_mfma_f32_16x16x32_bf16 v[36:39], v[56:59], v[84:87], v[36:39]\n\t" \
  "s_waitcnt vmcnt(10) lgkmcnt(0)\n\t" \
  "v_mfma_f32_16x16x32_bf16 v[0:3], v[60:63], v[88:91], v[0:3]\n\t" \
  "ds_read_b128 v[40:43], v147 offset:17408\n\t" \
  "v_mfma_f32_16x16x32_bf16 v[4:7], v[64:67], v[88:91], v[4:7]\n\t" \
  "ds_read_b128 v[44:47], v147 offset:18432\n\t" \
  "v_mfma_f32_16x16x32_bf16 v[8:11], v[68:71], v[88:91], v[8:11]\n\t" \
  "ds_read_b128 v[48:51], v147 offset:19456\n\t" \
  "v_mfma_f32_16x16x32_bf16 v[12:15], v[72:75], v[88:91], v[12:15]\n\t" \
  "ds_read_b128 v[52:55], v147 offset:20480\n\t" \
  "v_mfma_f32_16x16x32_bf16 v[16:19], v[76:79], v[88:91], v[16:19]\n\t" \
  "ds_read_b128 v[56:59], v147 offset:21504\n\t" \
  "v_mfma_f32_16x16x32_bf16 v[20:23], v[60:63], v[92:95], v[20:23]\n\t" \
  "buffer_load_dwordx4 v[136:139], v144, %[srd], %[so] offen sc1\n\t" \
  "v_mfma_f32_16x16x32_bf16 v[24:27], v[64:67], v[92:95], v[24:27]\n\t" \
  "buffer_load_dwordx4 v[140:143], v145, %[srd], %[so] offen sc1\n\t" \
  "s_add_u32 %[so], %[so], 8192\n\t" \
  "v_mfma_f32_16x16x32_bf16 v[28:31], v[68:71], v[92:95], v[28:31]\n\t" \
  "buffer_load_dwordx4 v[80:83], v144, %[srd], %[so] offen sc1\n\t" \
  "v_mfma_f32_16x16x32_bf16 v[32:35], v[72:75], v[92:95], v[32:35]\n\t" \
  "buffer_load_dwordx4 v[84:87], v145, %[srd], %[so] offen sc1\n\t" \
  "s_add_u32 %[so], %[so], 8192\n\t" \
  "v_mfma_f32_16x16x32_bf16 v[36:39], v[76:79], v[92:95], v[36:39]\n\t" \
  "s_waitcnt vmcnt(12) lgkmcnt(0)\n\t" \
  "v_mfma_f32_16x16x32_bf16 v[0:3], v[40:43], v[96:99], v[0:3]\n\t" \
  "ds_read_b128 v[60:63], v147 offset:27648\n\t" \
  "v_mfma_f32_16x16x32_bf16 v[4:7], v[44:47], v[96:99], v[4:7]\n\t" \
  "ds_read_b128 v[64:67], v147 offset:28672\n\t" \
  "v_mfma_f32_16x16x32_bf16 v[8:11], v[48:51], v[96:99], v[8:11]\n\t" \
  "ds_read_b128 v[68:71], v147 offset:29696\n\t" \
  "v_mfma_f32_16x16x32_bf16 v[12:15], v[52:55], v[96:99], v[12:15]\n\t" \
  "ds_read_b128 v[72:75], v147 offset:30720\n\t" \
  "v_mfma_f32_16x16x32_bf16 v[16:19], v[56:59], v[96:99], v[16:19]\n\t" \
  "ds_read_b128 v[76:79], v147 offset:31744\n\t" \
  "v_mfma_f32_16x16x32_bf16 v[20:23], v[40:43], v[100:103], v[20:23]\n\t" \
  "buffer_load_dwordx4 v[88:91], v144, %[srd], %[so] offen sc1\n\t" \
  "v_mfma_f32_16x16x32_bf16 v[24:27], v[44:47], v[100:103], v[24:27]\n\t" \
  "buffer_load_dwordx4 v[92:95], v145, %[srd], %[so] offen sc1\n\t" \
  "s_add_u32 %[so], %[so], 8192\n\t" \
  "v_mfma_f32_16x16x32_bf16 v[28:31], v[48:51], v[100:103], v[28:31]\n\t" \
  "v_mfma_f32_16x16x32_bf16 v[32:35], v[52:55], v[100:103], v[32:35]\n\t" \
  "v_mfma_f32_16x16x32_bf16 v[36:39], v[56:59], v[100:103], v[36:39]\n\t" \
  "s_waitcnt vmcnt(12) lgkmcnt(0)\n\t" \
  "v_mfma_f32_16x16x32_bf16 v[0:3], v[60:63], v[104:107], v[0:3]\n\t" \
  "ds_read_b128 v[40:43], v147 offset:37888\n\t" \
  "v_mfma_f32_16x16x32_bf16 v[4:7], v[64:67], v[104:107], v[4:7]\n\t" \
  "ds_read_b128 v[44:47], v147 offset:38912\n\t" \
  "v_mfma_f32_16x16x32_bf16 v[8:11], v[68:71], v[104:107], v[8:11]\n\t" \
  "ds_read_b128 v[48:51], v147 offset:39936\n\t" \
  "v_mfma_f32_16x16x32_bf16 v[12:15], v[72:75], v[104:107], v[12:15]\n\t" \
  "ds_read_b128 v[52:55], v147 offset:40960\n\t" \
  "v_mfma_f32_16x16x32_bf16 v[16:19], v[76:79], v[104:107], v[16:19]\n\t" \
  "ds_read_b128 v[56:59], v147 offset:41984\n\t" \
  "v_mfma_f32_16x16x32_bf16 v[20:23], v[60:63], v[108:111], v[20:23]\n\t" \
  "v_mfma_f32_16x16x32_bf16 v[24:27], v[64:67], v[108:111], v[24:27]\n\t" \
  "v_mfma_f32_16x16x32_bf16 v[28:31], v[68:71], v[108:111], v[28:31]\n\t" \
  "v_mfma_f32_16x16x32_bf16 v[32:35], v[72:75], v[108:111], v[32:35]\n\t" \
  "v_mfma_f32_16x16x32_bf16 v[36:39], v[76:79], v[108:111], v[36:39]\n\t" \
  "s_waitcnt vmcnt(10) lgkmcnt(0)\n\t" \
  "v_mfma_f32_16x16x32_bf16 v[0:3], v[40:43], v[112:115], v[0:3]\n\t" \
  "ds_read_b128 v[60:63], v147 offset:48128\n\t" \
  "v_mfma_f32_16x16x32_bf16 v[4:7], v[44:47], v[112:115], v[4:7]\n\t" \
  "ds_read_b128 v[64:67], v147 offset:49152\n\t" \
  "v_mfma_f32_16x16x32_bf16 v[8:11], v[48:51], v[112:115], v[8:11]\n\t" \
  "ds_read_b128 v[68:71], v147 offset:50176\n\t" \
  "v_mfma_f32_16x16x32_bf16 v[12:15], v[52:55], v[112:115], v[12:15]\n\t" \
  "ds_read_b128 v[72:75], v147 offset:51200\n\t" \
  "v_mfma_f32_16x16x32_bf16 v[16:19], v[56:59], v[112:115], v[16:19]\n\t" \
  "ds_read_b128 v[76:79], v147 offset:52224\n\t" \
  "v_mfma_f32_16x16x32_bf16 v[20:23], v[40:43], v[116:119], v[20:23]\n\t" \
  "v_mfma_f32_16x16x32_bf16 v[24:27], v[44:47], v[116:119], v[24:27]\n\t" \
  "v_mfma_f32_16x16x32_bf16 v[28:31], v[48:51], v[116:119], v[28:31]\n\t" \
  "v_mfma_f32_16x16x32_bf16 v[32:35], v[52:55], v[116:119], v[32:35]\n\t" \
  "v_mfma_f32_16x16x32_bf16 v[36:39], v[56:59], v[116:119], v[36:39]\n\t" \
  "s_waitcnt vmcnt(8) lgkmcnt(0)\n\t" \
  "v_mfma_f32_16x16x32_bf16 v[0:3], v[60:63], v[120:123], v[0:3]\n\t" \
  "ds_read_b128 v[40:43], v147 offset:58368\n\t" \
  "v_mfma_f32_16x16x32_bf16 v[4:7], v[64:67], v[120:123], v[4:7]\n\t" \
  "ds_read_b128 v[44:47], v147 offset:59392\n\t" \
  "v_mfma_f32_16x16x32_bf16 v[8:11], v[68:71], v[120:123], v[8:11]\n\t" \
  "ds_read_b128 v[48:51], v147 offset:60416\n\t" \
  "v_mfma_f32_16x16x32_bf16 v[12:15], v[72:75], v[120:123], v[12:15]\n\t" \
  "ds_read_b128 v[52:55], v147 offset:61440\n\t" \
  "v_mfma_f32_16x16x32_bf16 v[16:19], v[76:79], v[120:123], v[16:19]\n\t" \
  "ds_read_b128 v[56:59], v147 offset:62464\n\t" \
  "v_mfma_f32_16x16x32_bf16 v[20:23], v[60:63], v[124:127], v[20:23]\n\t" \
  "v_mfma_f32_16x16x32_bf16 v[24:27], v[64:67], v[124:127], v[24:27]\n\t" \
  "v_mfma_f32_16x16x32_bf16 v[28:31], v[68:71], v[124:127], v[28:31]\n\t" \
  "v_mfma_f32_16x16x32_bf16 v[32:35], v[72:75], v[124:127], v[32:35]\n\t" \
  "v_mfma_f32_16x16x32_bf16 v[36:39], v[76:79], v[124:127], v[36:39]\n\t" \
  "s_waitcnt vmcnt(6) lgkmcnt(0)\n\t" \
  "v_mfma_f32_16x16x32_bf16 v[0:3], v[40:43], v[128:131], v[0:3]\n\t" \
  "ds_read_b128 v[60:63], v148 offset:7168\n\t" \
  "v_mfma_f32_16x16x32_bf16 v[4:7], v[44:47], v[128:131], v[4:7]\n\t" \
  "ds_read_b128 v[64:67], v148 offset:8192\n\t" \
  "v_mfma_f32_16x16x32_bf16 v[8:11], v[48:51], v[128:131], v[8:11]\n\t" \
  "ds_read_b128 v[68:71], v148 offset:9216\n\t" \
  "v_mfma_f32_16x16x32_bf16 v[12:15], v[52:55], v[128:131], v[12:15]\n\t" \
  "ds_read_b128 v[72:75], v148 offset:10240\n\t" \
  "v_mfma_f32_16x16x32_bf16 v[16:19], v[56:59], v[128:131], v[16:19]\n\t" \
  "ds_read_b128 v[76:79], v148 offset:11264\n\t" \
  "v_mfma_f32_16x16x32_bf16 v[20:23], v[40:43], v[132:135], v[20:23]\n\t" \
  "v_mfma_f32_16x16x32_bf16 v[24:27], v[44:47], v[132:135], v[24:27]\n\t" \
  "v_mfma_f32_16x16x32_bf16 v[28:31], v[48:51], v[132:135], v[28:31]\n\t" \
  "v_mfma_f32_16x16x32_bf16 v[32:35], v[52:55], v[132:135], v[32:35]\n\t" \
  "v_mfma_f32_16x16x32_bf16 v[36:39], v[56:59], v[132:135], v[36:39]\n\t" \
  "s_waitcnt vmcnt(4) lgkmcnt(0)\n\t" \
  "v_mfma_f32_16x16x32_bf16 v[0:3], v[60:63], v[136:139], v[0:3]\n\t" \
  "ds_read_b128 v[40:43], v148 offset:17408\n\t" \
  "v_mfma_f32_16x16x32_bf16 v[4:7], v[64:67], v[136:139], v[4:7]\n\t" \
  "ds_read_b128 v[44:47], v148 offset:18432\n\t" \
  "v_mfma_f32_16x16x32_bf16 v[8:11], v[68:71], v[136:139], v[8:11]\n\t" \
  "ds_read_b128 v[48:51], v148 offset:19456\n\t" \
  "v_mfma_f32_16x16x32_bf16 v[12:15], v[72:75], v[136:139], v[12:15]\n\t" \
  "ds_read_b128 v[52:55], v148 offset:20480\n\t" \
  "v_mfma_f32_16x16x32_bf16 v[16:19], v[76:79], v[136:139], v[16:19]\n\t" \
  "ds_read_b128 v[56:59], v148 offset:21504\n\t" \
  "v_mfma_f32_16x16x32_bf16 v[20:23], v[60:63], v[140:143], v[20:23]\n\t" \
  "v_mfma_f32_16x16x32_bf16 v[24:27], v[64:67], v[140:143], v[24:27]\n\t" \
  "v_mfma_f32_16x16x32_bf16 v[28:31], v[68:71], v[140:143], v[28:31]\n\t" \
  "v_mfma_f32_16x16x32_bf16 v[32:35], v[72:75], v[140:143], v[32:35]\n\t" \
  "v_mfma_f32_16x16x32_bf16 v[36:39], v[76:79], v[140:143], v[36:39]\n\t" \
  "s_waitcnt vmcnt(2) lgkmcnt(0)\n\t" \
  "v_mfma_f32_16x16x32_bf16 v[0:3], v[40:43], v[80:83], v[0:3]\n\t" \
  "ds_read_b128 v[60:63], v148 offset:27648\n\t" \
  "v_mfma_f32_16x16x32_bf16 v[4:7], v[44:47], v[80:83], v[4:7]\n\t" \
  "ds_read_b128 v[64:67], v148 offset:28672\n\t" \
  "v_mfma_f32_16x16x32_bf16 v[8:11], v[48:51], v[80:83], v[8:11]\n\t" \
  "ds_read_b128 v[68:71], v148 offset:29696\n\t" \
  "v_mfma_f32_16x16x32_bf16 v[12:15], v[52:55], v[80:83], v[12:15]\n\t" \
  "ds_read_b128 v[72:75], v148 offset:30720\n\t" \
  "v_mfma_f32_16x16x32_bf16 v[16:19], v[56:59], v[80:83], v[16:19]\n\t" \
  "ds_read_b128 v[76:79], v148 offset:31744\n\t" \
  "v_mfma_f32_16x16x32_bf16 v[20:23], v[40:43], v[84:87], v[20:23]\n\t" \
  "v_mfma_f32_16x16x32_bf16 v[24:27], v[44:47], v[84:87], v[24:27]\n\t" \
  "v_mfma_f32_16x16x32_bf16 v[28:31], v[48:51], v[84:87], v[28:31]\n\t" \
  "v_mfma_f32_16x16x32_bf16 v[32:35], v[52:55], v[84:87], v[32:35]\n\t" \
  "v_mfma_f32_16x16x32_bf16 v[36:39], v[56:59], v[84:87], v[36:39]\n\t" \
  "s_waitcnt vmcnt(0) lgkmcnt(0)\n\t" \
  "s_barrier\n\t" \
  "v_mfma_f32_16x16x32_bf16 v[20:23], v[60:63], v[92:95], v[20:23]\n\t" \
  "v_mfma_f32_16x16x32_bf16 v[24:27], v[64:67], v[92:95], v[24:27]\n\t" \
  "v_mfma_f32_16x16x32_bf16 v[28:31], v[68:71], v[92:95], v[28:31]\n\t" \
  "v_mfma_f32_16x16x32_bf16 v[32:35], v[72:75], v[92:95], v[32:35]\n\t" \
  "v_mfma_f32_16x16x32_bf16 v[36:39], v[76:79], v[92:95], v[36:39]\n\t" \
  "v_mfma_f32_16x16x32_bf16 v[0:3], v[60:63], v[88:91], v[0:3]\n\t" \
  "v_mfma_f32_16x16x32_bf16 v[4:7], v[64:67], v[88:91], v[4:7]\n\t" \
  "v_mfma_f32_16x16x32_bf16 v[8:11], v[68:71], v[88:91], v[8:11]\n\t" \
  "v_mfma_f32_16x16x32_bf16 v[12:15], v[72:75], v[88:91], v[12:15]\n\t" \
  "ds_write_b128 v149, v[20:23] offset:0\n\t" \
  "v_mfma_f32_16x16x32_bf16 v[16:19], v[76:79], v[88:91], v[16:19]\n\t" \
  "ds_write_b128 v149, v[24:27] offset:1024\n\t" \
  "ds_write_b128 v149, v[28:31] offset:2048\n\t" \
  "ds_write_b128 v149, v[32:35] offset:3072\n\t" \
  "ds_write_b128 v149, v[36:39] offset:4096\n\t" \
  "s_waitcnt lgkmcnt(0)\n\t" \
  "s_barrier\n\t" \
  "ds_read_b128 v[20:23], v150 offset:0\n\t" \
  "ds_read_b128 v[24:27], v150 offset:1024\n\t" \
  "ds_read_b128 v[28:31], v150 offset:2048\n\t" \
  "ds_read_b128 v[32:35], v150 offset:3072\n\t" \
  "ds_read_b128 v[36:39], v150 offset:4096\n\t" \
  "s_waitcnt lgkmcnt(4)\n\t" \
  "v_pk_add_f32 v[0:1], v[0:1], v[20:21]\n\t" \
  "v_pk_add_f32 v[2:3], v[2:3], v[22:23]\n\t" \
  "s_waitcnt lgkmcnt(3)\n\t" \
  "v_pk_add_f32 v[4:5], v[4:5], v[24:25]\n\t" \
  "v_pk_add_f32 v[6:7], v[6:7], v[26:27]\n\t" \
  "s_waitcnt lgkmcnt(2)\n\t" \
  "v_pk_add_f32 v[8:9], v[8:9], v[28:29]\n\t" \
  "v_pk_add_f32 v[10:11], v[10:11], v[30:31]\n\t" \
  "s_waitcnt lgkmcnt(1)\n\t" \
  "v_pk_add_f32 v[12:13], v[12:13], v[32:33]\n\t" \
  "v_pk_add_f32 v[14:15], v[14:15], v[34:35]\n\t" \
  "s_waitcnt lgkmcnt(0)\n\t" \
  "v_pk_add_f32 v[16:17], v[16:17], v[36:37]\n\t" \
  "v_pk_add_f32 v[18:19], v[18:19], v[38:39]\n\t"
#define WO_CLOB "v40", "v41", "v42", "v43", "v44", "v45", "v46", "v47", "v48", "v49", "v50", "v51", "v52", "v53", "v54", "v55", "v56", "v57", "v58", "v59", "v60", "v61", "v62", "v63", "v64", "v65", "v66", "v67", "v68", "v69", "v70", "v71", "v72", "v73", "v74", "v75", "v76", "v77", "v78", "v79", "v80", "v81", "v82", "v83", "v84", "v85", "v86", "v87", "v88", "v89", "v90", "v91", "v92", "v93", "v94", "v95", "v96", "v97", "v98", "v99", "v100", "v101", "v102", "v103", "v104", "v105", "v106", "v107", "v108", "v109", "v110", "v111", "v112", "v113", "v114", "v115", "v116", "v117", "v118", "v119", "v120", "v121", "v122", "v123", "v124", "v125", "v126", "v127", "v128", "v129", "v130", "v131", "v132", "v133", "v134", "v135", "v136", "v137", "v138", "v139", "v140", "v141", "v142", "v143", "v147", "v148"
#define WO_AX0 "{v144}"
#define WO_AX1 "{v145}"
#define WO_AB "{v146}"
#define WO_AE0 "{v149}"
#define WO_AE1 "{v150}"
#define DN_ASM \
  "v_add_u32 v147, 61440, v146\n\t" \
  "v_add_u32 v148, 122880, v146\n\t" \
  "s_barrier\n\t" \
  "buffer_load_dwordx4 v[80:83], v144, %[srd], %[so] offen sc1\n\t" \
  "buffer_load_dwordx4 v[84:87], v145, %[srd], %[so] offen sc1\n\t" \
  "s_add_u32 %[so], %[so], 8192\n\t" \
  "buffer_load_dwordx4 v[88:91], v144, %[srd], %[so] offen sc1\n\t" \
  "buffer_load_dwordx4 v[92:95], v145, %[srd], %[so] offen sc1\n\t" \
  "s_add_u32 %[so], %[so], 8192\n\t" \
  "buffer_load_dwordx4 v[96:99], v144, %[srd], %[so] offen sc1\n\t" \
  "buffer_load_dwordx4 v[100:103], v145, %[srd], %[so] offen sc1\n\t" \
  "s_add_u32 %[so], %[so], 8192\n\t" \
  "buffer_load_dwordx4 v[104:107], v144, %[srd], %[so] offen sc1\n\t" \
  "buffer_load_dwordx4 v[108:111], v145, %[srd], %[so] offen sc1\n\t" \
  "s_add_u32 %[so], %[so], 8192\n\t" \
  "buffer_load_dwordx4 v[112:115], v144, %[srd], %[so] offen sc1\n\t" \
  "buffer_load_dwordx4 v[116:119], v145, %[srd], %[so] offen sc1\n\t" \
  "s_add_u32 %[so], %[so], 8192\n\t" \
  "ds_read_b128 v[40:43], v148 offset:27648\n\t" \
  "ds_read_b128 v[44:47], v148 offset:28672\n\t" \
  "ds_read_b128 v[48:51], v148 offset:29696\n\t" \
  "ds_read_b128 v[52:55], v148 offset:30720\n\t" \
  "ds_read_b128 v[56:59], v148 offset:31744\n\t" \
  "s_waitcnt vmcnt(8) lgkmcnt(0)\n\t" \
  "v_mfma_f32_16x16x32_bf16 v[0:3], v[40:43], v[80:83], v[0:3]\n\t" \
  "ds_read_b128 v[60:63], v146 offset:58368\n\t" \
  "v_mfma_f32_16x16x32_bf16 v[4:7], v[44:47], v[80:83], v[4:7]\n\t" \
  "ds_read_b128 v[64:67], v146 offset:59392\n\t" \
  "v_mfma_f32_16x16x32_bf16 v[8:11], v[48:51], v[80:83], v[8:11]\n\t" \
  "ds_read_b128 v[68:71], v146 offset:60416\n\t" \
  "v_mfma_f32_16x16x32_bf16 v[12:15], v[52:55], v[80:83], v[12:15]\n\t" \
  "ds_read_b128 v[72:75], v146 offset:61440\n\t" \
  "v_mfma_f32_16x16x32_bf16 v[16:19], v[56:59], v[80:83], v[16:19]\n\t" \
  "ds_read_b128 v[76:79], v146 offset:62464\n\t" \
  "v_mfma_f32_16x16x32_bf16 v[20:23], v[40:43], v[84:87], v[20:23]\n\t" \
  "buffer_load_dwordx4 v[120:123], v144, %[srd], %[so] offen sc1\n\t" \
  "v_mfma_f32_16x16x32_bf16 v[24:27], v[44:47], v[84:87], v[24:27]\n\t" \
  "buffer_load_dwordx4 v[124:127], v145, %[srd], %[so] offen sc1\n\t" \
  "s_add_u32 %[so], %[so], 8192\n\t" \
  "v_mfma_f32_16x16x32_bf16 v[28:31], v[48:51], v[84:87], v[28:31]\n\t" \
  "buffer_load_dwordx4 v[128:131], v144, %[srd], %[so] offen sc1\n\t" \
  "v_mfma_f32_16x16x32_bf16 v[32:35], v[52:55], v[84:87], v[32:35]\n\t" \
  "buffer_load_dwordx4 v[132:135], v145, %[srd], %[so] offen sc1\n\t" \
  "s_add_u32 %[so], %[so], 8192\n\t" \
  "v_mfma_f32_16x16x32_bf16 v[36:39], v[56:59], v[84:87], v[36:39]\n\t" \
  "s_waitcnt vmcnt(10) lgkmcnt(0)\n\t" \
  "v_mfma_f32_16x16x32_bf16 v[0:3], v[60:63], v[88:91], v[0:3]\n\t" \
  "ds_read_b128 v[40:43], v147 offset:7168\n\t" \
  "v_mfma_f32_16x16x32_bf16 v[4:7], v[64:67], v[88:91], v[4:7]\n\t" \
  "ds_read_b128 v[44:47], v147 offset:8192\n\t" \
  "v_mfma_f32_16x16x32_bf16 v[8:11], v[68:71], v[88:91], v[8:11]\n\t" \
  "ds_read_b128 v[48:51], v147 offset:9216\n\t" \
  "v_mfma_f32_16x16x32_bf16 v[12:15], v[72:75], v[88:91], v[12:15]\n\t" \
  "ds_read_b128 v[52:55], v147 offset:10240\n\t" \
  "v_mfma_f32_16x16x32_bf16 v[16:19], v[76:79], v[88:91], v[16:19]\n\t" \
  "ds_read_b128 v[56:59], v147 offset:11264\n\t" \
  "v_mfma_f32_16x16x32_bf16 v[20:23], v[60:63], v[92:95], v[20:23]\n\t" \
  "buffer_load_dwordx4 v[136:139], v144, %[srd], %[so] offen sc1\n\t" \
  "v_mfma_f32_16x16x32_bf16 v[24:27], v[64:67], v[92:95], v[24:27]\n\t" \
  "buffer_load_dwordx4 v[140:143], v145, %[srd], %[so] offen sc1\n\t" \
  "s_add_u32 %[so], %[so], 8192\n\t" \
  "v_mfma_f32_16x16x32_bf16 v[28:31], v[68:71], v[92:95], v[28:31]\n\t" \
  "buffer_load_dwordx4 v[80:83], v144, %[srd], %[so] offen sc1\n\t" \
  "v_mfma_f32_16x16x32_bf16 v[32:35], v[72:75], v[92:95], v[32:35]\n\t" \
  "buffer_load_dwordx4 v[84:87], v145, %[srd], %[so] offen sc1\n\t" \
  "s_add_u32 %[so], %[so], 8192\n\t" \
  "v_mfma_f32_16x16x32_bf16 v[36:39], v[76:79], v[92:95], v[36:39]\n\t" \
  "s_waitcnt vmcnt(12) lgkmcnt(0)\n\t" \
  "s_barrier\n\t" \
  "v_mfma_f32_16x16x32_bf16 v[0:3], v[40:43], v[96:99], v[0:3]\n\t" \
  "ds_read_b128 v[60:63], v147 offset:17408\n\t" \
  "v_mfma_f32_16x16x32_bf16 v[4:7], v[44:47], v[96:99], v[4:7]\n\t" \
  "ds_read_b128 v[64:67], v147 offset:18432\n\t" \
  "v_mfma_f32_16x16x32_bf16 v[8:11], v[48:51], v[96:99], v[8:11]\n\t" \
  "ds_read_b128 v[68:71], v147 offset:19456\n\t" \
  "v_mfma_f32_16x16x32_bf16 v[12:15], v[52:55], v[96:99], v[12:15]\n\t" \
  "ds_read_b128 v[72:75], v147 offset:20480\n\t" \
  "v_mfma_f32_16x16x32_bf16 v[16:19], v[56:59], v[96:99], v[16:19]\n\t" \
  "ds_read_b128 v[76:79], v147 offset:21504\n\t" \
  "v_mfma_f32_16x16x32_bf16 v[20:23], v[40:43], v[100:103], v[20:23]\n\t" \
  "buffer_load_dwordx4 v[88:91], v144, %[srd], %[so] offen sc1\n\t" \
  "v_mfma_f32_16x16x32_bf16 v[24:27], v[44:47], v[100:103], v[24:27]\n\t" \
  "buffer_load_dwordx4 v[92:95], v145, %[srd], %[so] offen sc1\n\t" \
  "s_add_u32 %[so], %[so], 8192\n\t" \
  "v_mfma_f32_16x16x32_bf16 v[28:31], v[48:51], v[100:103], v[28:31]\n\t" \
  "v_mfma_f32_16x16x32_bf16 v[32:35], v[52:55], v[100:103], v[32:35]\n\t" \
  "v_mfma_f32_16x16x32_bf16 v[36:39], v[56:59], v[100:103], v[36:39]\n\t" \
  "s_waitcnt vmcnt(12) lgkmcnt(0)\n\t" \
  "v_mfma_f32_16x16x32_bf16 v[0:3], v[60:63], v[104:107], v[0:3]\n\t" \
  "ds_read_b128 v[40:43], v147 offset:58368\n\t" \
  "v_mfma_f32_16x16x32_bf16 v[4:7], v[64:67], v[104:107], v[4:7]\n\t" \
  "ds_read_b128 v[44:47], v147 offset:59392\n\t" \
  "v_mfma_f32_16x16x32_bf16 v[8:11], v[68:71], v[104:107], v[8:11]\n\t" \
  "ds_read_b128 v[48:51], v147 offset:60416\n\t" \
  "v_mfma_f32_16x16x32_bf16 v[12:15], v[72:75], v[104:107], v[12:15]\n\t" \
  "ds_read_b128 v[52:55], v147 offset:61440\n\t" \
  "v_mfma_f32_16x16x32_bf16 v[16:19], v[76:79], v[104:107], v[16:19]\n\t" \
  "ds_read_b128 v[56:59], v147 offset:62464\n\t" \
  "v_mfma_f32_16x16x32_bf16 v[20:23], v[60:63], v[108:111], v[20:23]\n\t" \
  "buffer_load_dwordx4 v[96:99], v144, %[srd], %[so] offen sc1\n\t" \
  "v_mfma_f32_16x16x32_bf16 v[24:27], v[64:67], v[108:111], v[24:27]\n\t" \
  "buffer_load_dwordx4 v[100:103], v145, %[srd], %[so] offen sc1\n\t" \
  "s_add_u32 %[so], %[so], 8192\n\t" \
  "v_mfma_f32_16x16x32_bf16 v[28:31], v[68:71], v[108:111], v[28:31]\n\t" \
  "v_mfma_f32_16x16x32_bf16 v[32:35], v[72:75], v[108:111], v[32:35]\n\t" \
  "v_mfma_f32_16x16x32_bf16 v[36:39], v[76:79], v[108:111], v[36:39]\n\t" \
  "s_waitcnt vmcnt(12) lgkmcnt(0)\n\t" \
  "v_mfma_f32_16x16x32_bf16 v[0:3], v[40:43], v[112:115], v[0:3]\n\t" \
  "ds_read_b128 v[60:63], v146 offset:7168\n\t" \
  "v_mfma_f32_16x16x32_bf16 v[4:7], v[44:47], v[112:115], v[4:7]\n\t" \
  "ds_read_b128 v[64:67], v146 offset:8192\n\t" \
  "v_mfma_f32_16x16x32_bf16 v[8:11], v[48:51], v[112:115], v[8:11]\n\t" \
  "ds_read_b128 v[68:71], v146 offset:9216\n\t" \
  "v_mfma_f32_16x16x32_bf16 v[12:15], v[52:55], v[112:115], v[12:15]\n\t" \
  "ds_read_b128 v[72:75], v146 offset:10240\n\t" \
  "v_mfma_f32_16x16x32_bf16 v[16:19], v[56:59], v[112:115], v[16:19]\n\t" \
  "ds_read_b128 v[76:79], v146 offset:11264\n\t" \
  "v_mfma_f32_16x16x32_bf16 v[20:23], v[40:43], v[116:119], v[20:23]\n\t" \
  "buffer_load_dwordx4 v[104:107], v144, %[srd], %[so] offen sc1\n\t" \
  "v_mfma_f32_16x16x32_bf16 v[24:27], v[44:47], v[116:119], v[24:27]\n\t" \
  "buffer_load_dwordx4 v[108:111], v145, %[srd], %[so] offen sc1\n\t" \
  "s_add_u32 %[so], %[so], 8192\n\t" \
  "v_mfma_f32_16x16x32_bf16 v[28:31], v[48:51], v[116:119], v[28:31]\n\t" \
  "v_mfma_f32_16x16x32_bf16 v[32:35], v[52:55], v[116:119], v[32:35]\n\t" \
  "v_mfma_f32_16x16x32_bf16 v[36:39], v[56:59], v[116:119], v[36:39]\n\t" \
  "s_waitcnt vmcnt(12) lgkmcnt(0)\n\t" \
  "v_mfma_f32_16x16x32_bf16 v[0:3], v[60:63], v[120:123], v[0:3]\n\t" \
  "ds_read_b128 v[40:43], v148 offset:7168\n\t" \
  "v_mfma_f32_16x16x32_bf16 v[4:7], v[64:67], v[120:123], v[4:7]\n\t" \
  "ds_read_b128 v[44:47], v148 offset:8192\n\t" \
  "v_mfma_f32_16x16x32_bf16 v[8:11], v[68:71], v[120:123], v[8:11]\n\t" \
  "ds_read_b128 v[48:51], v148 offset:9216\n\t" \
  "v_mfma_f32_16x16x32_bf16 v[12:15], v[72:75], v[120:123], v[12:15]\n\t" \
  "ds_read_b128 v[52:55], v148 offset:10240\n\t" \
  "v_mfma_f32_16x16x32_bf16 v[16:19], v[76:79], v[120:123], v[16:19]\n\t" \
  "ds_read_b128 v[56:59], v148 offset:11264\n\t" \
  "v_mfma_f32_16x16x32_bf16 v[20:23], v[60:63], v[124:127], v[20:23]\n\t" \
  "buffer_load_dwordx4 v[112:115], v144, %[srd], %[so] offen sc1\n\t" \
  "v_mfma_f32_16x16x32_bf16 v[24:27], v[64:67], v[124:127], v[24:27]\n\t" \
  "buffer_load_dwordx4 v[116:119], v145, %[srd], %[so] offen sc1\n\t" \
  "s_add_u32 %[so], %[so], 8192\n\t" \
  "v_mfma_f32_16x16x32_bf16 v[28:31], v[68:71], v[124:127], v[28:31]\n\t" \
  "v_mfma_f32_16x16x32_bf16 v[32:35], v[72:75], v[124:127], v[32:35]\n\t" \
  "v_mfma_f32_16x16x32_bf16 v[36:39], v[76:79], v[124:127], v[36:39]\n\t" \
  "s_waitcnt vmcnt(12) lgkmcnt(0)\n\t" \
  "v_mfma_f32_16x16x32_bf16 v[0:3], v[40:43], v[128:131], v[0:3]\n\t" \
  "ds_read_b128 v[60:63], v148 offset:17408\n\t" \
  "v_mfma_f32_16x16x32_bf16 v[4:7], v[44:47], v[128:131], v[4:7]\n\t" \
  "ds_read_b128 v[64:67], v148 offset:18432\n\t" \
  "v_mfma_f32_16x16x32_bf16 v[8:11], v[48:51], v[128:131], v[8:11]\n\t" \
  "ds_read_b128 v[68:71], v148 offset:19456\n\t" \
  "v_mfma_f32_16x16x32_bf16 v[12:15], v[52:55], v[128:131], v[12:15]\n\t" \
  "ds_read_b128 v[72:75], v148 offset:20480\n\t" \
  "v_mfma_f32_16x16x32_bf16 v[16:19], v[56:59], v[128:131], v[16:19]\n\t" \
  "ds_read_b128 v[76:79], v148 offset:21504\n\t" \
  "v_mfma_f32_16x16x32_bf16 v[20:23], v[40:43], v[132:135], v[20:23]\n\t" \
  "buffer_load_dwordx4 v[120:123], v144, %[srd], %[so] offen sc1\n\t" \
  "v_mfma_f32_16x16x32_bf16 v[24:27], v[44:47], v[132:135], v[24:27]\n\t" \
  "buffer_load_dwordx4 v[124:127], v145, %[srd], %[so] offen sc1\n\t" \
  "s_add_u32 %[so], %[so], 8192\n\t" \
  "v_mfma_f32_16x16x32_bf16 v[28:31], v[48:51], v[132:135], v[28:31]\n\t" \
  "v_mfma_f32_16x16x32_bf16 v[32:35], v[52:55], v[132:135], v[32:35]\n\t" \
  "v_mfma_f32_16x16x32_bf16 v[36:39], v[56:59], v[132:135], v[36:39]\n\t" \
  "s_waitcnt vmcnt(12) lgkmcnt(0)\n\t" \
  "v_mfma_f32_16x16x32_bf16 v[0:3], v[60:63], v[136:139], v[0:3]\n\t" \
  "ds_read_b128 v[40:43], v146 offset:17408\n\t" \
  "v_mfma_f32_16x16x32_bf16 v[4:7], v[64:67], v[136:139], v[4:7]\n\t" \
  "ds_read_b128 v[44:47], v146 offset:18432\n\t" \
  "v_mfma_f32_16x16x32_bf16 v[8:11], v[68:71], v[136:139], v[8:11]\n\t" \
  "ds_read_b128 v[48:51], v146 offset:19456\n\t" \
  "v_mfma_f32_16x16x32_bf16 v[12:15], v[72:75], v[136:139], v[12:15]\n\t" \
  "ds_read_b128 v[52:55], v146 offset:20480\n\t" \
  "v_mfma_f32_16x16x32_bf16 v[16:19], v[76:79], v[136:139], v[16:19]\n\t" \
  "ds_read_b128 v[56:59], v146 offset:21504\n\t" \
  "v_mfma_f32_16x16x32_bf16 v[20:23], v[60:63], v[140:143], v[20:23]\n\t" \
  "buffer_load_dwordx4 v[128:131], v144, %[srd], %[so] offen sc1\n\t" \
  "v_mfma_f32_16x16x32_bf16 v[24:27], v[64:67], v[140:143], v[24:27]\n\t" \
  "buffer_load_dwordx4 v[132:135], v145, %[srd], %[so] offen sc1\n\t" \
  "s_add_u32 %[so], %[so], 8192\n\t" \
  "v_mfma_f32_16x16x32_bf16 v[28:31], v[68:71], v[140:143], v[28:31]\n\t" \
  "v_mfma_f32_16x16x32_bf16 v[32:35], v[72:75], v[140:143], v[32:35]\n\t" \
  "v_mfma_f32_16x16x32_bf16 v[36:39], v[76:79], v[140:143], v[36:39]\n\t" \
  "s_waitcnt vmcnt(12) lgkmcnt(0)\n\t" \
  "s_barrier\n\t" \
  "v_mfma_f32_16x16x32_bf16 v[0:3], v[40:43], v[80:83], v[0:3]\n\t" \
  "ds_read_b128 v[60:63], v146 offset:27648\n\t" \
  "v_mfma_f32_16x16x32_bf16 v[4:7], v[44:47], v[80:83], v[4:7]\n\t" \
  "ds_read_b128 v[64:67], v146 offset:28672\n\t" \
  "v_mfma_f32_16x16x32_bf16 v[8:11], v[48:51], v[80:83], v[8:11]\n\t" \
  "ds_read_b128 v[68:71], v146 offset:29696\n\t" \
  "v_mfma_f32_16x16x32_bf16 v[12:15], v[52:55], v[80:83], v[12:15]\n\t" \
  "ds_read_b128 v[72:75], v146 offset:30720\n\t" \
  "v_mfma_f32_16x16x32_bf16 v[16:19], v[56:59], v[80:83], v[16:19]\n\t" \
  "ds_read_b128 v[76:79], v146 offset:31744\n\t" \
  "v_mfma_f32_16x16x32_bf16 v[20:23], v[40:43], v[84:87], v[20:23]\n\t" \
  "buffer_load_dwordx4 v[136:139], v144, %[srd], %[so] offen sc1\n\t" \
  "v_mfma_f32_16x16x32_bf16 v[24:27], v[44:47], v[84:87], v[24:27]\n\t" \
  "buffer_load_dwordx4 v[140:143], v145, %[srd], %[so] offen sc1\n\t" \
  "s_add_u32 %[so], %[so], 8192\n\t" \
  "v_mfma_f32_16x16x32_bf16 v[28:31], v[48:51], v[84:87], v[28:31]\n\t" \
  "v_mfma_f32_16x16x32_bf16 v[32:35], v[52:55], v[84:87], v[32:35]\n\t" \
  "v_mfma_f32_16x16x32_bf16 v[36:39], v[56:59], v[84:87], v[36:39]\n\t" \
  "s_waitcnt vmcnt(12) lgkmcnt(0)\n\t" \
  "v_mfma_f32_16x16x32_bf16 v[0:3], v[60:63], v[88:91], v[0:3]\n\t" \
  "ds_read_b128 v[40:43], v146 offset:37888\n\t" \
  "v_mfma_f32_16x16x32_bf16 v[4:7], v[64:67], v[88:91], v[4:7]\n\t" \
  "ds_read_b128 v[44:47], v146 offset:38912\n\t" \
  "v_mfma_f32_16x16x32_bf16 v[8:11], v[68:71], v[88:91], v[8:11]\n\t" \
  "ds_read_b128 v[48:51], v146 offset:39936\n\t" \
  "v_mfma_f32_16x16x32_bf16 v[12:15], v[72:75], v[88:91], v[12:15]\n\t" \
  "ds_read_b128 v[52:55], v146 offset:40960\n\t" \
  "v_mfma_f32_16x16x32_bf16 v[16:19], v[76:79], v[88:91], v[16:19]\n\t" \
  "ds_read_b128 v[56:59], v146 offset:41984\n\t" \
  "v_mfma_f32_16x16x32_bf16 v[20:23], v[60:63], v[92:95], v[20:23]\n\t" \
  "buffer_load_dwordx4 v[80:83], v144, %[srd], %[so] offen sc1\n\t" \
  "v_mfma_f32_16x16x32_bf16 v[24:27], v[64:67], v[92:95], v[24:27]\n\t" \
  "buffer_load_dwordx4 v[84:87], v145, %[srd], %[so] offen sc1\n\t" \
  "s_add_u32 %[so], %[so], 8192\n\t" \
  "v_mfma_f32_16x16x32_bf16 v[28:31], v[68:71], v[92:95], v[28:31]\n\t" \
  "v_mfma_f32_16x16x32_bf16 v[32:35], v[72:75], v[92:95], v[32:35]\n\t" \
  "v_mfma_f32_16x16x32_bf16 v[36:39], v[76:79], v[92:95], v[36:39]\n\t" \
  "s_waitcnt vmcnt(12) lgkmcnt(0)\n\t" \
  "v_mfma_f32_16x16x32_bf16 v[0:3], v[40:43], v[96:99], v[0:3]\n\t" \
  "ds_read_b128 v[60:63], v146 offset:48128\n\t" \
  "v_mfma_f32_16x16x32_bf16 v[4:7], v[44:47], v[96:99], v[4:7]\n\t" \
  "ds_read_b128 v[64:67], v146 offset:49152\n\t" \
  "v_mfma_f32_16x16x32_bf16 v[8:11], v[48:51], v[96:99], v[8:11]\n\t" \
  "ds_read_b128 v[68:71], v146 offset:50176\n\t" \
  "v_mfma_f32_16x16x32_bf16 v[12:15], v[52:55], v[96:99], v[12:15]\n\t" \
  "ds_read_b128 v[72:75], v146 offset:51200\n\t" \
  "v_mfma_f32_16x16x32_bf16 v[16:19], v[56:59], v[96:99], v[16:19]\n\t" \
  "ds_read_b128 v[76:79], v146 offset:52224\n\t" \
  "v_mfma_f32_16x16x32_bf16 v[20:23], v[40:43], v[100:103], v[20:23]\n\t" \
  "buffer_load_dwordx4 v[88:91], v144, %[srd], %[so] offen sc1\n\t" \
  "v_mfma_f32_16x16x32_bf16 v[24:27], v[44:47], v[100:103], v[24:27]\n\t" \
  "buffer_load_dwordx4 v[92:95], v145, %[srd], %[so] offen sc1\n\t" \
  "s_add_u32 %[so], %[so], 8192\n\t" \
  "v_mfma_f32_16x16x32_bf16 v[28:31], v[48:51], v[100:103], v[28:31]\n\t" \
  "v_mfma_f32_16x16x32_bf16 v[32:35], v[52:55], v[100:103], v[32:35]\n\t" \
  "v_mfma_f32_16x16x32_bf16 v[36:39], v[56:59], v[100:103], v[36:39]\n\t" \
  "s_waitcnt vmcnt(12) lgkmcnt(0)\n\t" \
  "v_mfma_f32_16x16x32_bf16 v[0:3], v[60:63], v[104:107], v[0:3]\n\t" \
  "ds_read_b128 v[40:43], v147 offset:27648\n\t" \
  "v_mfma_f32_16x16x32_bf16 v[4:7], v[64:67], v[104:107], v[4:7]\n\t" \
  "ds_read_b128 v[44:47], v147 offset:28672\n\t" \
  "v_mfma_f32_16x16x32_bf16 v[8:11], v[68:71], v[104:107], v[8:11]\n\t" \
  "ds_read_b128 v[48:51], v147 offset:29696\n\t" \
  "v_mfma_f32_16x16x32_bf16 v[12:15], v[72:75], v[104:107], v[12:15]\n\t" \
  "ds_read_b128 v[52:55], v147 offset:30720\n\t" \
  "v_mfma_f32_16x16x32_bf16 v[16:19], v[76:79], v[104:107], v[16:19]\n\t" \
  "ds_read_b128 v[56:59], v147 offset:31744\n\t" \
  "v_mfma_f32_16x16x32_bf16 v[20:23], v[60:63], v[108:111], v[20:23]\n\t" \
  "buffer_load_dwordx4 v[96:99], v144, %[srd], %[so] offen sc1\n\t" \
  "v_mfma_f32_16x16x32_bf16 v[24:27], v[64:67], v[108:111], v[24:27]\n\t" \
  "buffer_load_dwordx4 v[100:103], v145, %[srd], %[so] offen sc1\n\t" \
  "s_add_u32 %[so], %[so], 8192\n\t" \
  "v_mfma_f32_16x16x32_bf16 v[28:31], v[68:71], v[108:111], v[28:31]\n\t" \
  "v_mfma_f32_16x16x32_bf16 v[32:35], v[72:75], v[108:111], v[32:35]\n\t" \
  "v_mfma_f32_16x16x32_bf16 v[36:39], v[76:79], v[108:111], v[36:39]\n\t" \
  "s_waitcnt vmcnt(12) lgkmcnt(0)\n\t" \
  "v_mfma_f32_16x16x32_bf16 v[0:3], v[40:43], v[112:115], v[0:3]\n\t" \
  "ds_read_b128 v[60:63], v147 offset:37888\n\t" \
  "v_mfma_f32_16x16x32_bf16 v[4:7], v[44:47], v[112:115], v[4:7]\n\t" \
  "ds_read_b128 v[64:67], v147 offset:38912\n\t" \
  "v_mfma_f32_16x16x32_bf16 v[8:11], v[48:51], v[112:115], v[8:11]\n\t" \
  "ds_read_b128 v[68:71], v147 offset:39936\n\t" \
  "v_mfma_f32_16x16x32_bf16 v[12:15], v[52:55], v[112:115], v[12:15]\n\t" \
  "ds_read_b128 v[72:75], v147 offset:40960\n\t" \
  "v_mfma_f32_16x16x32_bf16 v[16:19], v[56:59], v[112:115], v[16:19]\n\t" \
  "ds_read_b128 v[76:79], v147 offset:41984\n\t" \
  "v_mfma_f32_16x16x32_bf16 v[20:23], v[40:43], v[116:119], v[20:23]\n\t" \
  "buffer_load_dwordx4 v[104:107], v144, %[srd], %[so] offen sc1\n\t" \
  "v_mfma_f32_16x16x32_bf16 v[24:27], v[44:47], v[116:119], v[24:27]\n\t" \
  "buffer_load_dwordx4 v[108:111], v145, %[srd], %[so] offen sc1\n\t" \
  "s_add_u32 %[so], %[so], 8192\n\t" \
  "v_mfma_f32_16x16x32_bf16 v[28:31], v[48:51], v[116:119], v[28:31]\n\t" \
  "v_mfma_f32_16x16x32_bf16 v[32:35], v[52:55], v[116:119], v[32:35]\n\t" \
  "v_mfma_f32_16x16x32_bf16 v[36:39], v[56:59], v[116:119], v[36:39]\n\t" \
  "s_waitcnt vmcnt(12) lgkmcnt(0)\n\t" \
  "v_mfma_f32_16x16x32_bf16 v[0:3], v[60:63], v[120:123], v[0:3]\n\t" \
  "ds_read_b128 v[40:43], v147 offset:48128\n\t" \
  "v_mfma_f32_16x16x32_bf16 v[4:7], v[64:67], v[120:123], v[4:7]\n\t" \
  "ds_read_b128 v[44:47], v147 offset:49152\n\t" \
  "v_mfma_f32_16x16x32_bf16 v[8:11], v[68:71], v[120:123], v[8:11]\n\t" \
  "ds_read_b128 v[48:51], v147 offset:50176\n\t" \
  "v_mfma_f32_16x16x32_bf16 v[12:15], v[72:75], v[120:123], v[12:15]\n\t" \
  "ds_read_b128 v[52:55], v147 offset:51200\n\t" \
  "v_mfma_f32_16x16x32_bf16 v[16:19], v[76:79], v[120:123], v[16:19]\n\t" \
  "ds_read_b128 v[56:59], v147 offset:52224\n\t" \
  "v_mfma_f32_16x16x32_bf16 v[20:23], v[60:63], v[124:127], v[20:23]\n\t" \
  "buffer_load_dwordx4 v[112:115], v144, %[srd], %[so] offen sc1\n\t" \
  "v_mfma_f32_16x16x32_bf16 v[24:27], v[64:67], v[124:127], v[24:27]\n\t" \
  "buffer_load_dwordx4 v[116:119], v145, %[srd], %[so] offen sc1\n\t" \
  "s_add_u32 %[so], %[so], 8192\n\t" \
  "v_mfma_f32_16x16x32_bf16 v[28:31], v[68:71], v[124:127], v[28:31]\n\t" \
  "v_mfma_f32_16x16x32_bf16 v[32:35], v[72:75], v[124:127], v[32:35]\n\t" \
  "v_mfma_f32_16x16x32_bf16 v[36:39], v[76:79], v[124:127], v[36:39]\n\t" \
  "s_waitcnt vmcnt(12) lgkmcnt(0)\n\t" \
  "s_barrier\n\t" \
  "v_mfma_f32_16x16x32_bf16 v[0:3], v[40:43], v[128:131], v[0:3]\n\t" \
  "ds_read_b128 v[60:63], v148 offset:27648\n\t" \
  "v_mfma_f32_16x16x32_bf16 v[4:7], v[44:47], v[128:131], v[4:7]\n\t" \
  "ds_read_b128 v[64:67], v148 offset:28672\n\t" \
  "v_mfma_f32_16x16x32_bf16 v[8:11], v[48:51], v[128:131], v[8:11]\n\t" \
  "ds_read_b128 v[68:71], v148 offset:29696\n\t" \
  "v_mfma_f32_16x16x32_bf16 v[12:15], v[52:55], v[128:131], v[12:15]\n\t" \
  "ds_read_b128 v[72:75], v148 offset:30720\n\t" \
  "v_mfma_f32_16x16x32_bf16 v[16:19], v[56:59], v[128:131], v[16:19]\n\t" \
  "ds_read_b128 v[76:79], v148 offset:31744\n\t" \
  "v_mfma_f32_16x16x32_bf16 v[20:23], v[40:43], v[132:135], v[20:23]\n\t" \
  "buffer_load_dwordx4 v[120:123], v144, %[srd], %[so] offen sc1\n\t" \
  "v_mfma_f32_16x16x32_bf16 v[24:27], v[44:47], v[132:135], v[24:27]\n\t" \
  "buffer_load_dwordx4 v[124:127], v145, %[srd], %[so] offen sc1\n\t" \
  "s_add_u32 %[so], %[so], 8192\n\t" \
  "v_mfma_f32_16x16x32_bf16 v[28:31], v[48:51], v[132:135], v[28:31]\n\t" \
  "v_mfma_f32_16x16x32_bf16 v[32:35], v[52:55], v[132:135], v[32:35]\n\t" \
  "v_mfma_f32_16x16x32_bf16 v[36:39], v[56:59], v[132:135], v[36:39]\n\t" \
  "s_waitcnt vmcnt(12) lgkmcnt(0)\n\t" \
  "v_mfma_f32_16x16x32_bf16 v[0:3], v[60:63], v[136:139], v[0:3]\n\t" \
  "ds_read_b128 v[40:43], v146 offset:58368\n\t" \
  "v_mfma_f32_16x16x32_bf16 v[4:7], v[64:67], v[136:139], v[4:7]\n\t" \
  "ds_read_b128 v[44:47], v146 offset:59392\n\t" \
  "v_mfma_f32_16x16x32_bf16 v[8:11], v[68:71], v[136:139], v[8:11]\n\t" \
  "ds_read_b128 v[48:51], v146 offset:60416\n\t" \
  "v_mfma_f32_16x16x32_bf16 v[12:15], v[72:75], v[136:139], v[12:15]\n\t" \
  "ds_read_b128 v[52:55], v146 offset:61440\n\t" \
  "v_mfma_f32_16x16x32_bf16 v[16:19], v[76:79], v[136:139], v[16:19]\n\t" \
  "ds_read_b128 v[56:59], v146 offset:62464\n\t" \
  "v_mfma_f32_16x16x32_bf16 v[20:23], v[60:63], v[140:143], v[20:23]\n\t" \
  "buffer_load_dwordx4 v[128:131], v144, %[srd], %[so] offen sc1\n\t" \
  "v_mfma_f32_16x16x32_bf16 v[24:27], v[64:67], v[140:143], v[24:27]\n\t" \
  "buffer_load_dwordx4 v[132:135], v145, %[srd], %[so] offen sc1\n\t" \
  "s_add_u32 %[so], %[so], 8192\n\t" \
  "v_mfma_f32_16x16x32_bf16 v[28:31], v[68:71], v[140:143], v[28:31]\n\t" \
  "v_mfma_f32_16x16x32_bf16 v[32:35], v[72:75], v[140:143], v[32:35]\n\t" \
  "v_mfma_f32_16x16x32_bf16 v[36:39], v[76:79], v[140:143], v[36:39]\n\t" \
  "s_waitcnt vmcnt(12) lgkmcnt(0)\n\t" \
  "v_mfma_f32_16x16x32_bf16 v[0:3], v[40:43], v[80:83], v[0:3]\n\t" \
  "ds_read_b128 v[60:63], v147 offset:7168\n\t" \
  "v_mfma_f32_16x16x32_bf16 v[4:7], v[44:47], v[80:83], v[4:7]\n\t" \
  "ds_read_b128 v[64:67], v147 offset:8192\n\t" \
  "v_mfma_f32_16x16x32_bf16 v[8:11], v[48:51], v[80:83], v[8:11]\n\t" \
  "ds_read_b128 v[68:71], v147 offset:9216\n\t" \
  "v_mfma_f32_16x16x32_bf16 v[12:15], v[52:55], v[80:83], v[12:15]\n\t" \
  "ds_read_b128 v[72:75], v147 offset:10240\n\t" \
  "v_mfma_f32_16x16x32_bf16 v[16:19], v[56:59], v[80:83], v[16:19]\n\t" \
  "ds_read_b128 v[76:79], v147 offset:11264\n\t" \
  "v_mfma_f32_16x16x32_bf16 v[20:23], v[40:43], v[84:87], v[20:23]\n\t" \
  "buffer_load_dwordx4 v[136:139], v144, %[srd], %[so] offen sc1\n\t" \
  "v_mfma_f32_16x16x32_bf16 v[24:27], v[44:47], v[84:87], v[24:27]\n\t" \
  "buffer_load_dwordx4 v[140:143], v145, %[srd], %[so] offen sc1\n\t" \
  "s_add_u32 %[so], %[so], 8192\n\t" \
  "v_mfma_f32_16x16x32_bf16 v[28:31], v[48:51], v[84:87], v[28:31]\n\t" \
  "v_mfma_f32_16x16x32_bf16 v[32:35], v[52:55], v[84:87], v[32:35]\n\t" \
  "v_mfma_f32_16x16x32_bf16 v[36:39], v[56:59], v[84:87], v[36:39]\n\t" \
  "s_waitcnt vmcnt(12) lgkmcnt(0)\n\t" \
  "v_mfma_f32_16x16x32_bf16 v[0:3], v[60:63], v[88:91], v[0:3]\n\t" \
  "ds_read_b128 v[40:43], v147 offset:17408\n\t" \
  "v_mfma_f32_16x16x32_bf16 v[4:7], v[64:67], v[88:91], v[4:7]\n\t" \
  "ds_read_b128 v[44:47], v147 offset:18432\n\t" \
  "v_mfma_f32_16x16x32_bf16 v[8:11], v[68:71], v[88:91], v[8:11]\n\t" \
  "ds_read_b128 v[48:51], v147 offset:19456\n\t" \
  "v_mfma_f32_16x16x32_bf16 v[12:15], v[72:75], v[88:91], v[12:15]\n\t" \
  "ds_read_b128 v[52:55], v147 offset:20480\n\t" \
  "v_mfma_f32_16x16x32_bf16 v[16:19], v[76:79], v[88:91], v[16:19]\n\t" \
  "ds_read_b128 v[56:59], v147 offset:21504\n\t" \
  "v_mfma_f32_16x16x32_bf16 v[20:23], v[60:63], v[92:95], v[20:23]\n\t" \
  "v_mfma_f32_16x16x32_bf16 v[24:27], v[64:67], v[92:95], v[24:27]\n\t" \
  "v_mfma_f32_16x16x32_bf16 v[28:31], v[68:71], v[92:95], v[28:31]\n\t" \
  "v_mfma_f32_16x16x32_bf16 v[32:35], v[72:75], v[92:95], v[32:35]\n\t" \
  "v_mfma_f32_16x16x32_bf16 v[36:39], v[76:79], v[92:95], v[36:39]\n\t" \
  "s_waitcnt vmcnt(10) lgkmcnt(0)\n\t" \
  "v_mfma_f32_16x16x32_bf16 v[0:3], v[40:43], v[96:99], v[0:3]\n\t" \
  "ds_read_b128 v[60:63], v147 offset:58368\n\t" \
  "v_mfma_f32_16x16x32_bf16 v[4:7], v[44:47], v[96:99], v[4:7]\n\t" \
  "ds_read_b128 v[64:67], v147 offset:59392\n\t" \
  "v_mfma_f32_16x16x32_bf16 v[8:11], v[48:51], v[96:99], v[8:11]\n\t" \
  "ds_read_b128 v[68:71], v147 offset:60416\n\t" \
  "v_mfma_f32_16x16x32_bf16 v[12:15], v[52:55], v[96:99], v[12:15]\n\t" \
  "ds_read_b128 v[72:75], v147 offset:61440\n\t" \
  "v_mfma_f32_16x16x32_bf16 v[16:19], v[56:59], v[96:99], v[16:19]\n\t" \
  "ds_read_b128 v[76:79], v147 offset:62464\n\t" \
  "v_mfma_f32_16x16x32_bf16 v[20:23], v[40:43], v[100:103], v[20:23]\n\t" \
  "v_mfma_f32_16x16x32_bf16 v[24:27], v[44:47], v[100:103], v[24:27]\n\t" \
  "v_mfma_f32_16x16x32_bf16 v[28:31], v[48:51], v[100:103], v[28:31]\n\t" \
  "v_mfma_f32_16x16x32_bf16 v[32:35], v[52:55], v[100:103], v[32:35]\n\t" \
  "v_mfma_f32_16x16x32_bf16 v[36:39], v[56:59], v[100:103], v[36:39]\n\t" \
  "s_waitcnt vmcnt(8) lgkmcnt(0)\n\t" \
  "v_mfma_f32_16x16x32_bf16 v[0:3], v[60:63], v[104:107], v[0:3]\n\t" \
  "ds_read_b128 v[40:43], v146 offset:7168\n\t" \
  "v_mfma_f32_16x16x32_bf16 v[4:7], v[64:67], v[104:107], v[4:7]\n\t" \
  "ds_read_b128 v[44:47], v146 offset:8192\n\t" \
  "v_mfma_f32_16x16x32_bf16 v[8:11], v[68:71], v[104:107], v[8:11]\n\t" \
  "ds_read_b128 v[48:51], v146 offset:9216\n\t" \
  "v_mfma_f32_16x16x32_bf16 v[12:15], v[72:75], v[104:107], v[12:15]\n\t" \
  "ds_read_b128 v[52:55], v146 offset:10240\n\t" \
  "v_mfma_f32_16x16x32_bf16 v[16:19], v[76:79], v[104:107], v[16:19]\n\t" \
  "ds_read_b128 v[56:59], v146 offset:11264\n\t" \
  "v_mfma_f32_16x16x32_bf16 v[20:23], v[60:63], v[108:111], v[20:23]\n\t" \
  "v_mfma_f32_16x16x32_bf16 v[24:27], v[64:67], v[108:111], v[24:27]\n\t" \
  "v_mfma_f32_16x16x32_bf16 v[28:31], v[68:71], v[108:111], v[28:31]\n\t" \
  "v_mfma_f32_16x16x32_bf16 v[32:35], v[72:75], v[108:111], v[32:35]\n\t" \
  "v_mfma_f32_16x16x32_bf16 v[36:39], v[76:79], v[108:111], v[36:39]\n\t" \
  "s_waitcnt vmcnt(6) lgkmcnt(0)\n\t" \
  "s_barrier\n\t" \
  "v_mfma_f32_16x16x32_bf16 v[0:3], v[40:43], v[112:115], v[0:3]\n\t" \
  "ds_read_b128 v[60:63], v148 offset:7168\n\t" \
  "v_mfma_f32_16x16x32_bf16 v[4:7], v[44:47], v[112:115], v[4:7]\n\t" \
  "ds_read_b128 v[64:67], v148 offset:8192\n\t" \
  "v_mfma_f32_16x16x32_bf16 v[8:11], v[48:51], v[112:115], v[8:11]\n\t" \
  "ds_read_b128 v[68:71], v148 offset:9216\n\t" \
  "v_mfma_f32_16x16x32_bf16 v[12:15], v[52:55], v[112:115], v[12:15]\n\t" \
  "ds_read_b128 v[72:75], v148 offset:10240\n\t" \
  "v_mfma_f32_16x16x32_bf16 v[16:19], v[56:59], v[112:115], v[16:19]\n\t" \
  "ds_read_b128 v[76:79], v148 offset:11264\n\t" \
  "v_mfma_f32_16x16x32_bf16 v[20:23], v[40:43], v[116:119], v[20:23]\n\t" \
  "v_mfma_f32_16x16x32_bf16 v[24:27], v[44:47], v[116:119], v[24:27]\n\t" \
  "v_mfma_f32_16x16x32_bf16 v[28:31], v[48:51], v[116:119], v[28:31]\n\t" \
  "v_mfma_f32_16x16x32_bf16 v[32:35], v[52:55], v[116:119], v[32:35]\n\t" \
  "v_mfma_f32_16x16x32_bf16 v[36:39], v[56:59], v[116:119], v[36:39]\n\t" \
  "s_waitcnt vmcnt(4) lgkmcnt(0)\n\t" \
  "v_mfma_f32_16x16x32_bf16 v[0:3], v[60:63], v[120:123], v[0:3]\n\t" \
  "ds_read_b128 v[40:43], v148 offset:17408\n\t" \
  "v_mfma_f32_16x16x32_bf16 v[4:7], v[64:67], v[120:123], v[4:7]\n\t" \
  "ds_read_b128 v[44:47], v148 offset:18432\n\t" \
  "v_mfma_f32_16x16x32_bf16 v[8:11], v[68:71], v[120:123], v[8:11]\n\t" \
  "ds_read_b128 v[48:51], v148 offset:19456\n\t" \
  "v_mfma_f32_16x16x32_bf16 v[12:15], v[72:75], v[120:123], v[12:15]\n\t" \
  "ds_read_b128 v[52:55], v148 offset:20480\n\t" \
  "v_mfma_f32_16x16x32_bf16 v[16:19], v[76:79], v[120:123], v[16:19]\n\t" \
  "ds_read_b128 v[56:59], v148 offset:21504\n\t" \
  "v_mfma_f32_16x16x32_bf16 v[20:23], v[60:63], v[124:127], v[20:23]\n\t" \
  "v_mfma_f32_16x16x32_bf16 v[24:27], v[64:67], v[124:127], v[24:27]\n\t" \
  "v_mfma_f32_16x16x32_bf16 v[28:31], v[68:71], v[124:127], v[28:31]\n\t" \
  "v_mfma_f32_16x16x32_bf16 v[32:35], v[72:75], v[124:127], v[32:35]\n\t" \
  "v_mfma_f32_16x16x32_bf16 v[36:39], v[76:79], v[124:127], v[36:39]\n\t" \
  "s_waitcnt vmcnt(2) lgkmcnt(0)\n\t" \
  "v_mfma_f32_16x16x32_bf16 v[0:3], v[40:43], v[128:131], v[0:3]\n\t" \
  "ds_read_b128 v[60:63], v146 offset:17408\n\t" \
  "v_mfma_f32_16x16x32_bf16 v[4:7], v[44:47], v[128:131], v[4:7]\n\t" \
  "ds_read_b128 v[64:67], v146 offset:18432\n\t" \
  "v_mfma_f32_16x16x32_bf16 v[8:11], v[48:51], v[128:131], v[8:11]\n\t" \
  "ds_read_b128 v[68:71], v146 offset:19456\n\t" \
  "v_mfma_f32_16x16x32_bf16 v[12:15], v[52:55], v[128:131], v[12:15]\n\t" \
  "ds_read_b128 v[72:75], v146 offset:20480\n\t" \
  "v_mfma_f32_16x16x32_bf16 v[16:19], v[56:59], v[128:131], v[16:19]\n\t" \
  "ds_read_b128 v[76:79], v146 offset:21504\n\t" \
  "v_mfma_f32_16x16x32_bf16 v[20:23], v[40:43], v[132:135], v[20:23]\n\t" \
  "v_mfma_f32_16x16x32_bf16 v[24:27], v[44:47], v[132:135], v[24:27]\n\t" \
  "v_mfma_f32_16x16x32_bf16 v[28:31], v[48:51], v[132:135], v[28:31]\n\t" \
  "v_mfma_f32_16x16x32_bf16 v[32:35], v[52:55], v[132:135], v[32:35]\n\t" \
  "v_mfma_f32_16x16x32_bf16 v[36:39], v[56:59], v[132:135], v[36:39]\n\t" \
  "s_waitcnt vmcnt(0) lgkmcnt(0)\n\t" \
  "s_barrier\n\t" \
  "v_mfma_f32_16x16x32_bf16 v[20:23], v[60:63], v[140:143], v[20:23]\n\t" \
  "v_mfma_f32_16x16x32_bf16 v[24:27], v[64:67], v[140:143], v[24:27]\n\t" \
  "v_mfma_f32_16x16x32_bf16 v[28:31], v[68:71], v[140:143], v[28:31]\n\t" \
  "v_mfma_f32_16x16x32_bf16 v[32:35], v[72:75], v[140:143], v[32:35]\n\t" \
  "v_mfma_f32_16x16x32_bf16 v[36:39], v[76:79], v[140:143], v[36:39]\n\t" \
  "v_mfma_f32_16x16x32_bf16 v[0:3], v[60:63], v[136:139], v[0:3]\n\t" \
  "v_mfma_f32_16x16x32_bf16 v[4:7], v[64:67], v[136:139], v[4:7]\n\t" \
  "v_mfma_f32_16x16x32_bf16 v[8:11], v[68:71], v[136:139], v[8:11]\n\t" \
  "v_mfma_f32_16x16x32_bf16 v[12:15], v[72:75], v[136:139], v[12:15]\n\t" \
  "ds_write_b128 v149, v[20:23] offset:0\n\t" \
  "v_mfma_f32_16x16x32_bf16 v[16:19], v[76:79], v[136:139], v[16:19]\n\t" \
  "ds_write_b128 v149, v[24:27] offset:1024\n\t" \
  "ds_write_b128 v149, v[28:31] offset:2048\n\t" \
  "ds_write_b128 v149, v[32:35] offset:3072\n\t" \
  "ds_write_b128 v149, v[36:39] offset:4096\n\t" \
  "s_waitcnt lgkmcnt(0)\n\t" \
  "s_barrier\n\t" \
  "ds_read_b128 v[20:23], v150 offset:0\n\t" \
  "ds_read_b128 v[24:27], v150 offset:1024\n\t" \
  "ds_read_b128 v[28:31], v150 offset:2048\n\t" \
  "ds_read_b128 v[32:35], v150 offset:3072\n\t" \
  "ds_read_b128 v[36:39], v150 offset:4096\n\t" \
  "s_waitcnt lgkmcnt(4)\n\t" \
  "v_pk_add_f32 v[0:1], v[0:1], v[20:21]\n\t" \
  "v_pk_add_f32 v[2:3], v[2:3], v[22:23]\n\t" \
  "s_waitcnt lgkmcnt(3)\n\t" \
  "v_pk_add_f32 v[4:5], v[4:5], v[24:25]\n\t" \
  "v_pk_add_f32 v[6:7], v[6:7], v[26:27]\n\t" \
  "s_waitcnt lgkmcnt(2)\n\t" \
  "v_pk_add_f32 v[8:9], v[8:9], v[28:29]\n\t" \
  "v_pk_add_f32 v[10:11], v[10:11], v[30:31]\n\t" \
  "s_waitcnt lgkmcnt(1)\n\t" \
  "v_pk_add_f32 v[12:13], v[12:13], v[32:33]\n\t" \
  "v_pk_add_f32 v[14:15], v[14:15], v[34:35]\n\t" \
  "s_waitcnt lgkmcnt(0)\n\t" \
  "v_pk_add_f32 v[16:17], v[16:17], v[36:37]\n\t" \
  "v_pk_add_f32 v[18:19], v[18:19], v[38:39]\n\t"
#define DN_CLOB "v40", "v41", "v42", "v43", "v44", "v45", "v46", "v47", "v48", "v49", "v50", "v51", "v52", "v53", "v54", "v55", "v56", "v57", "v58", "v59", "v60", "v61", "v62", "v63", "v64", "v65", "v66", "v67", "v68", "v69", "v70", "v71", "v72", "v73", "v74", "v75", "v76", "v77", "v78", "v79", "v80", "v81", "v82", "v83", "v84", "v85", "v86", "v87", "v88", "v89", "v90", "v91", "v92", "v93", "v94", "v95", "v96", "v97", "v98", "v99", "v100", "v101", "v102", "v103", "v104", "v105", "v106", "v107", "v108", "v109", "v110", "v111", "v112", "v113", "v114", "v115", "v116", "v117", "v118", "v119", "v120", "v121", "v122", "v123", "v124", "v125", "v126", "v127", "v128", "v129", "v130", "v131", "v132", "v133", "v134", "v135", "v136", "v137", "v138", "v139", "v140", "v141", "v142", "v143", "v147", "v148"
#define DN_AX0 "{v144}"
#define DN_AX1 "{v145}"
#define DN_AB "{v146}"
#define DN_AE0 "{v149}"
#define DN_AE1 "{v150}"
constexpr int DN_SLOT[15] = {14, 5, 6, 7, 11, 0, 12, 13, 1, 2, 3, 4, 8, 9, 10};

constexpr unsigned SZ_ACT = 2048u * 640u * 2u;
constexpr unsigned SZ_ST  = 2048u * 64u;
constexpr unsigned KVT = 2688u, QTL = 1280u;
constexpr unsigned SZ_KV = 32u * 64u * KVT, SZ_H = 2048u * 1536u * 2u;
constexpr unsigned MB_X = 0, MB_WQ = 1, MB_Q = 2, MB_A = 3, MB_W = 4, MB_G = 5, MB_D = 6, MB_LAYER = 5, MB_WORDS = 8192;
constexpr unsigned FP_O = 11 * MB_WORDS, FP_G = FP_O + 512, FP_D = FP_G + 1024, FL_EPOCH = FP_D + 512, FL_TMO = FL_EPOCH + 32;
constexpr unsigned OFF_CSQ = 753664;
constexpr unsigned OFF_CSG = OFF_CSQ + 960u * 256u;
constexpr unsigned OFF_WQ = 1310720;
constexpr unsigned OFF_WO = OFF_WQ + 16u * 163840u;
constexpr unsigned OFF_WG = OFF_WO + 8u * 102400u;
constexpr unsigned OFF_WD = OFF_WG + 16u * 245760u;
constexpr unsigned OFF_X  = OFF_WD + 8u * 245760u;
constexpr unsigned OFF_SX = OFF_X + 2u * SZ_ACT;
#define REUSE 14
constexpr unsigned OFF_X2 = OFF_SX + 2u * SZ_ST;
constexpr unsigned OFF_S2 = OFF_X2 + 2u * SZ_ACT;
constexpr unsigned OFF_Q  = OFF_SX + 4u * SZ_ST + 2u * SZ_ACT;
constexpr unsigned LS_ACT = (REUSE & 1) ? 0 : SZ_ACT, LS_ST = (REUSE & 1) ? 0 : SZ_ST, LS_KV = (REUSE & 2) ? 0 : SZ_KV, LS_Q = (REUSE & 2) ? 0 : SZ_ACT, LS_O = (REUSE & 4) ? 0 : SZ_ACT, LS_H = (REUSE & 8) ? 0 : SZ_H;
constexpr unsigned OFF_KV = OFF_Q + 2u * SZ_ACT;
constexpr unsigned OFF_O  = OFF_KV + 2u * SZ_KV;
constexpr unsigned OFF_H  = OFF_O + 2u * SZ_ACT;
constexpr unsigned OFF_DBG = OFF_H + 2u * SZ_H;
constexpr unsigned WS_USED = OFF_DBG + 256u * 128u * 12u;
constexpr unsigned LDS_BYTES = 163840, L_MISC = 160768, L_MU = L_MISC, L_RS = L_MISC + 512, L_CSG = L_MISC + 1024, L_CSQ = L_MISC + 1792,
                   L_VERD = L_MISC + 2332, L_CNT = L_MISC + 2336, L_GO = L_MISC + 2340, L_GO2 = L_MISC + 2344, L_CNT2 = L_MISC + 2348, L_JUNK = L_MISC + 2352;
constexpr unsigned L_RING = 15360, ASTG = 4u * KVT, L_WB = 58368, STGW = 8192;
constexpr int CODE_LINES = 11;
constexpr float QSCALE = 0.22360679774997896f * 1.4426950408889634f;

DI unsigned f2u(float v) { return __builtin_bit_cast(unsigned, v); }
DI float u2f(unsigned v) { return __builtin_bit_cast(float, v); }
DI unsigned pk2(f32x2 v) { return __builtin_bit_cast(unsigned, __builtin_convertvector(v, bf16x2)); }
DI unsigned pk2(float lo, float hi) { return pk2(f32x2{lo, hi}); }
DI float bflo(unsigned u) { return u2f(u << 16); }
DI float bfhi(unsigned u) { return u2f(u & 0xffff0000u); }
DI f32x2 lo2(f32x4 v) { return f32x2{v[0], v[1]}; }
DI f32x2 hi2(f32x4 v) { return f32x2{v[2], v[3]}; }
DI float xadd16(float v) { auto r = __builtin_amdgcn_permlane16_swap(f2u(v), f2u(v), false, false); return u2f(r[0]) + u2f(r[1]); }
DI float xadd32(float v) { auto r = __builtin_amdgcn_permlane32_swap(f2u(v), f2u(v), false, false); return u2f(r[0]) + u2f(r[1]); }
DI float xmax32(float v) { auto r = __builtin_amdgcn_permlane32_swap(f2u(v), f2u(v), false, false); return fmaxf(u2f(r[0]), u2f(r[1])); }

#define BAR() asm volatile("s_barrier" ::: "memory")
#define LBAR() asm volatile("s_waitcnt lgkmcnt(0)\n\ts_barrier" ::: "memory")
#define PIN(x) asm volatile("" : "+v"(x))
struct Ctx { rsrc_t srd; char* ws; u64 ep; LAS char* sh; unsigned shb; int w, lane, x, j; };
struct In { const char *x, *wqkv, *wo, *wg, *wu, *wd, *out; };

DI int row16(const Ctx& c) { return (c.x >> 2) * 8 + (c.j & 7); }
DI int col16(const Ctx& c) { return (c.x & 3) * 4 + (c.j >> 3); }
DI int row32(const Ctx& c) { return (c.x >> 1) * 8 + (c.j & 7); }
DI int col8(const Ctx& c) { return (c.x & 1) * 4 + (c.j >> 3); }
DI Ctx fresh(const Ctx& c0) {
  Ctx c = c0;
  asm volatile("" : "+s"(c.w), "+s"(c.x), "+s"(c.j), "+s"(c.shb));
  unsigned l; asm volatile("v_mbcnt_lo_u32_b32 %0, -1, 0\n\tv_mbcnt_hi_u32_b32 %0, -1, %0" : "=v"(l));
  c.lane = l; c.sh = (LAS char*)(uintptr_t)c.shb;
  return c;
}
template <int N> DI void wait_vm() {
  asm volatile("s_waitcnt vmcnt(%0)" :: "i"(N) : "memory");
  if (N == 0) __builtin_amdgcn_s_waitcnt(0x0F70);
}
template <int N> DI void dma(const Ctx& c, unsigned so, unsigned lds) {
  if constexpr (N > 4) { dma<4>(c, so, lds); dma<N - 4>(c, so + 4096, lds + 4096); }
  else {
    const unsigned vo = c.lane * 16;
#define DMA1(O) "buffer_load_dwordx4 %0, %1, %2 offen offset:" #O " sc1 lds\n\t"
#define DMAQ(T) asm volatile("s_mov_b32 m0, %3\n\ts_nop 3\n\t" T :: "v"(vo), "s"(c.srd), "s"(so), "s"(lds) : "memory")
    if constexpr (N == 1) DMAQ(DMA1(0)); else if constexpr (N == 2) DMAQ(DMA1(0) DMA1(1024));
    else if constexpr (N == 3) DMAQ(DMA1(0) DMA1(1024) DMA1(2048)); else DMAQ(DMA1(0) DMA1(1024) DMA1(2048) DMA1(3072));
  }
}
DI void st16(const Ctx& c, u32x4 v, unsigned vo) { __builtin_amdgcn_raw_buffer_store_b128(v, c.srd, vo, 0, SC1); }
DI void st8(const Ctx& c, u32x2 v, unsigned vo) { __builtin_amdgcn_raw_buffer_store_b64(v, c.srd, vo, 0, SC1); }
DI void st4(const Ctx& c, float v, unsigned vo) { __builtin_amdgcn_raw_buffer_store_b32(f2u(v), c.srd, vo, 0, SC1); }
DI u32x4 ld16(const Ctx& c, unsigned vo, unsigned so) { return __builtin_amdgcn_raw_buffer_load_b128(c.srd, vo, so, SC1); }
DI u32x2 ld8(const Ctx& c, unsigned vo, unsigned so) { return __builtin_amdgcn_raw_buffer_load_b64(c.srd, vo, so, SC1); }
DI void flag_store(const Ctx& c, unsigned word) { __hip_atomic_store((GAS u64*)c.ws + word, c.ep, RLX_AGENT); }
DI unsigned mb_word(unsigned mb, int consumer, int producer) { return mb * MB_WORDS + consumer * 32 + producer; }
DI void stores_issued(const Ctx& c) {
  asm volatile("s_mov_b64 exec, 1\n\tds_add_u32 %0, %1\n\ts_mov_b64 exec, -1" :: "v"(c.shb + L_CNT2), "v"(1u) : "memory");
}
template <int YOUNGER = 0> DI void arrive(const Ctx& c, unsigned target, int n, unsigned mb, int consumer) {
  const unsigned vo = mb_word(mb, consumer, 0) * 8;
  unsigned old, so;
  asm volatile("s_waitcnt vmcnt(%7)\n\ts_mov_b64 exec, 1\n\tds_add_rtn_u32 %0, %2, %3\n\ts_waitcnt lgkmcnt(0)\n\t"
               "v_readfirstlane_b32 %1, %0\n\ts_mov_b64 exec, %8\n\ts_cmp_lg_u32 %1, %6\n\ts_cbranch_scc1 Larr%=\n\tbuffer_atomic_add %3, %4, %5, 0 offen\n\t"
               "Larr%=:\n\ts_mov_b64 exec, -1"
               : "=&v"(old), "=&s"(so) : "v"(c.shb + L_CNT), "v"(1u), "v"(vo), "s"(c.srd), "s"(target - 1), "i"(YOUNGER), "s"((1ull << n) - 1) : "scc", "memory");
}
DI void lds_wait(const Ctx& c, unsigned off, unsigned v) {
#pragma nounroll
  for (unsigned spins = 0; spins < (1u << 22); ++spins) {
    if ((unsigned)__builtin_amdgcn_readfirstlane(__hip_atomic_load((LAS unsigned*)(c.sh + off), __ATOMIC_RELAXED, __HIP_MEMORY_SCOPE_WORKGROUP)) >= v) break;
    __builtin_amdgcn_s_sleep(1);
  }
  asm volatile("" ::: "memory");
}
DI void lds_set(const Ctx& c, unsigned off, unsigned v) { asm volatile("ds_write_b32 %0, %1" :: "v"(c.shb + off), "v"(v) : "memory"); }
template <int SL> DI void wait_word(const Ctx& c, unsigned word) {
  GAS u64* f = (GAS u64*)c.ws + word;
  GAS u64* tmo = (GAS u64*)c.ws + FL_TMO;
  for (unsigned spins = 0;;) {
    const u64 v = __hip_atomic_load(f, RLX_AGENT);
    if (__all(v == c.ep)) break;
    __builtin_amdgcn_s_sleep(SL);
    if ((++spins & 255u) == 0u) {
      if (__all(__hip_atomic_load(tmo, RLX_AGENT) == c.ep)) break;
      if (spins > (1u << 18)) { if (c.lane == 0) __hip_atomic_store(tmo, c.ep, RLX_AGENT); break; }
    }
  }
  asm volatile("" ::: "memory");
}

template <int SL = 1> DI void wait_flags(const Ctx& c, unsigned word, int n) { wait_word<SL>(c, word + (c.lane < n ? c.lane : 0)); }
DI void wait_count(const Ctx& c, unsigned word, unsigned n) {
  unsigned spins = 0;
  asm volatile("s_mov_b64 exec, 1\n\tv_mov_b32 v254, 0\n\tbuffer_atomic_add v254, %1, %2, 0 offen sc0\n\t"
               "Lp0_%=:\n\ts_sleep %4\n\tv_mov_b32 v255, 0\n\tbuffer_atomic_add v255, %1, %2, 0 offen sc0\n\ts_waitcnt vmcnt(1)\n\t"
               "v_cmp_ne_u32 vcc, %3, v254\n\ts_cbranch_vccz Lp1_%=\n\t"
               "s_sleep %4\n\tv_mov_b32 v254, 0\n\tbuffer_atomic_add v254, %1, %2, 0 offen sc0\n\ts_waitcnt vmcnt(1)\n\t"
               "v_cmp_ne_u32 vcc, %3, v255\n\ts_cbranch_vccz Lp1_%=\n\t"
               "s_add_u32 %0, %0, 1\n\ts_cmp_lt_u32 %0, 0x20000\n\ts_cbranch_scc1 Lp0_%=\n\t"
               "Lp1_%=:\n\ts_mov_b64 exec, -1"
               : "+s"(spins) : "v"(word * 8), "s"(c.srd), "s"(n), "i"(2) : "v254", "v255", "vcc", "scc", "memory");
  if (spins >= 0x20000 && c.lane == 0) __hip_atomic_store((GAS u64*)c.ws + FL_TMO, c.ep, RLX_AGENT);
}
template <int SL = 1> DI void await_inputs(const Ctx& c, unsigned done, unsigned mb, int consumer, int n) {
  wait_vm<0>();
  if (c.w == 4) {
    lds_wait(c, L_CNT, done);
    if (mb >= MB_Q) wait_count(c, mb_word(mb, consumer, 0), n);
    else { __builtin_amdgcn_s_sleep(10); wait_flags<SL>(c, mb_word(mb, consumer, 0), n); }
    lds_set(c, L_GO, done + 1);
  }
  else lds_wait(c, L_GO, done + 1);
}

template <int CH, int ROWS, int IT> DI void unit_load(u32x4 (&v)[IT], const char* mat, unsigned bytes, unsigned off, unsigned row_bytes, unsigned lane) {
  constexpr unsigned RPI = 64 / CH;
  const rsrc_t s = __builtin_amdgcn_make_buffer_rsrc((void*)mat, 0, bytes, 0x00020000);
  const unsigned r = lane / CH, vo = r < RPI ? r * row_bytes + (lane % CH) * 16 : 0x80000000u, vl = r < ROWS - (ROWS - 1) / RPI * RPI ? vo : 0x80000000u;
#pragma unroll
  for (unsigned i = 0; i < IT; ++i) v[i] = __builtin_amdgcn_raw_buffer_load_b128(s, (i + 1) * RPI <= ROWS ? vo : vl, off + (i * RPI < ROWS ? i : 0) * RPI * row_bytes, 0);
}
template <int CH, int ROWS, int STR, int IT> DI void unit_stage(const u32x4 (&v)[IT], LAS char* sh, unsigned lane) {
  constexpr unsigned RPI = 64 / CH;
  LAS char* d = sh + (lane / CH) * STR + (lane % CH) * 16;
  wait_vm<0>();
  if (lane < RPI * CH) {
#pragma unroll
    for (unsigned i = 0; i * RPI < ROWS; ++i) *(LAS u32x4*)(d + i * RPI * STR) = v[i];
  }
}
template <int STR> DI u32x4 unit_cell(LAS char* sh, int rA, int rB, int col, int lane) {
  const int q = (lane >> 2) & 3;
  s16x4 a = __builtin_amdgcn_ds_read_tr16_b64_v4i16((LAS s16x4*)(sh + (rA + q) * STR + col * 2));
  s16x4 b = __builtin_amdgcn_ds_read_tr16_b64_v4i16((LAS s16x4*)(sh + (rB + q) * STR + col * 2));
  u32x2 ua = __builtin_bit_cast(u32x2, a), ub = __builtin_bit_cast(u32x2, b);
  return u32x4{ua[0], ua[1], ub[0], ub[1]};
}
DI float cell_sum(u32x4 v, float s) {
  const bf16x2 one = {(__bf16)1.0f, (__bf16)1.0f};
#pragma unroll
  for (int i = 0; i < 4; ++i) { const unsigned u = v[i]; s = __builtin_amdgcn_fdot2_f32_bf16(__builtin_bit_cast(bf16x2, u), one, s, false); }
  return s;
}
DI float cell_sq(u32x4 v, float s) {
#pragma unroll
  for (int i = 0; i < 4; ++i) { const unsigned u = v[i]; s = __builtin_amdgcn_fdot2_f32_bf16(__builtin_bit_cast(bf16x2, u), __builtin_bit_cast(bf16x2, u), s, false); }
  return s;
}
DI int wd_colgrp(int blk, int p) { return blk < 4 ? 32 * (blk >> 1) + 8 * p + 4 * (blk & 1) : 64 + 4 * p; }

DI void touch(const Ctx& c, unsigned vo, unsigned so) {
  asm volatile("s_mov_b32 m0, %3\n\ts_nop 0\n\tbuffer_load_dword %0, %1, %2 offen sc1 lds" :: "v"(vo), "s"(c.srd), "s"(so), "s"(c.shb + L_JUNK) : "memory");
}
DI void set_epoch(Ctx& c, u64 e0) {
  c.ep = (((u64)(unsigned)__builtin_amdgcn_readfirstlane((unsigned)(e0 >> 32)) << 32) | (unsigned)__builtin_amdgcn_readfirstlane((unsigned)e0)) + 1;
}
DI void prep_first(Ctx c, const In& in, u64 e0) {
  const int q = c.lane >> 4, s16 = c.lane & 15, p = c.lane & 3;
  LAS char* sh = c.sh + c.w * STGW;
  if (c.w < 2) {
    const int u = (c.x * 32 + c.j) * 2 + c.w, tok = (u >> 2) * 16 + s16, o0 = 20 * (u & 3) + q;
    u32x4 v[5];
#pragma unroll
    for (int m = 0; m < 5; ++m) v[m] = __builtin_amdgcn_raw_buffer_load_b128(__builtin_amdgcn_make_buffer_rsrc((void*)in.x, 0, 2048 * 1280, 0x00020000), tok * 1280 + o0 * 16 + m * 64, 0, 0);
    float s = 0.f, sq = 0.f;
#pragma unroll
    for (int m = 0; m < 5; ++m) st16(c, v[m], OFF_X + (tok >> 6) * 81920 + (o0 + 4 * m) * 1024 + (tok & 63) * 16);
    float s2 = 0.f, sq2 = 0.f;
#pragma unroll
    for (int m = 0; m < 5; ++m) { PIN(v[m]); if (m & 1) { s2 = cell_sum(v[m], s2); sq2 = cell_sq(v[m], sq2); } else { s = cell_sum(v[m], s); sq = cell_sq(v[m], sq); } }
    s = xadd32(xadd16(s + s2)); sq = xadd32(xadd16(sq + sq2));
    if (c.lane < 32) st8(c, c.lane < 16 ? u32x2{f2u(s), f2u(sq)} : u32x2{0, 0}, OFF_SX + (u >> 2) * 1024 + (u & 3) * 256 + c.lane * 8);
    wait_vm<0>();
    LBAR();
    PIN(e0); set_epoch(c, e0);
    if (c.w == 0 && c.lane < 16) flag_store(c, mb_word(MB_X, (u >> 5) * 16 + c.lane, (u >> 1) & 15));
  } else if (c.w != 5 && c.w != 7) {
    const int u = c.j & 15, t = 2 * c.x + (c.j >> 4), rho = u % 3, kk = (u / 3) * 4 + (c.w < 5 ? c.w - 2 : 3);
    if (u != 15) {
      u32x4 v[3];
      unit_load<5, 32>(v, in.wqkv, 640 * 3840, kk * 32 * 3840 + rho * 1280 + t * 80, 3840, c.lane);
      unit_stage<5, 32, 96>(v, sh, c.lane);
      float rec = 0.f;
#pragma unroll
      for (int i = 0; i < 3; ++i) {
        int col, gblk, slot = s16; bool act = true, zero = false;
        if (rho < 2) {
          if (i < 2) { col = 20 * (p >> 1) + 8 * (p & 1) + 4 * i; gblk = 2 * rho + i; }
          else { col = 20 * (p & 1) + 16; gblk = 4; act = s16 < 8; slot = 8 * rho + (s16 & 7); }
        } else { const int sg = 16 * i + 4 * p; col = sg < 40 ? sg : 0; gblk = 5 + i; zero = 16 * i + s16 >= 40; }
        u32x4 e = unit_cell<96>(sh, 8 * q, 8 * q + 4, col, c.lane);
        if (zero) e = u32x4{0, 0, 0, 0};
        st16(c, e, act ? OFF_WQ + t * 163840 + kk * 8192 + gblk * 1024 + q * 256 + slot * 16 : 0x80000000u);
        const float cs = xadd32(xadd16(cell_sum(e, 0.f)));
        if (q == i) rec = cs;
      }
      st4(c, rec, OFF_CSQ + ((t * 3 + rho) * 20 + kk) * 256 + c.lane * 4);
      wait_vm<0>();
    }
    LBAR();
    PIN(e0); set_epoch(c, e0);
    if (c.w == 2 && c.lane < 16) flag_store(c, mb_word(MB_WQ, c.lane * 16 + t, u));
  } else if (c.w == 5 && c.j < 16) {
    const int ct = c.j & 7, hp = c.x * 2 + (c.j >> 3);
    u32x4 v[7];
    unit_load<10, 40>(v, in.wo, 640 * 1280, hp * 40 * 1280 + ct * 160, 1280, c.lane);
    LBAR();
    unit_stage<10, 40, 176>(v, sh, c.lane);
#pragma unroll
    for (int it = 0; it < 7; ++it) {
      int qi = it * 4 + q; const bool act = qi < 25; if (!act) qi = 24;
      const int o = qi / 5, blk = qi % 5, rA = o < 4 ? 20 * (o >> 1) + 4 * (o & 1) : 16, rB = rA + (o < 4 ? 8 : 20), og = 5 * hp + o;
      const u32x4 e = unit_cell<176>(sh, rA, rB, wd_colgrp(blk, p), c.lane);
      if (act) st16(c, e, OFF_WO + ct * 102400 + (og >> 2) * 5120 + blk * 1024 + (og & 3) * 256 + s16 * 16);
    }
    wait_vm<0>();
    PIN(e0); set_epoch(c, e0);
    if (c.lane == 0) flag_store(c, FP_O + ct * 64 + hp);
  } else if (c.w == 7) {
    const unsigned kq = row16(c) * 16 + col16(c), ka = c.x * 32 + c.j, kw = row32(c) * 8 + col8(c);
    auto owner = [&](unsigned m) { return ((0x253u >> m) & 1) ? kq : ((0x84u >> m) & 1) ? ka : kw; };
    const unsigned m = MB_Q + (c.lane >> 3);
    st16(c, u32x4{0, 0, 0, 0}, mb_word(m, owner(m), (c.lane & 7) * 2) * 8);
    st16(c, u32x4{0, 0, 0, 0}, c.lane < 8 ? mb_word(10, kw, c.lane * 2) * 8 : 0x80000000u);
#pragma unroll
    for (int k = 0; k < 2; ++k) st16(c, u32x4{0, 0, 0, 0}, OFF_O + ((c.j & 7) * 4 + ((c.j >> 3) & 1) * 2 + k) * 81920 + (5 * (2 * c.x + (c.j >> 4)) + 4) * 1024 + c.lane * 16);
    touch(c, c.lane < 2 ? mb_word(c.lane, kq, 0) * 8 : 0x80000000u, 0);
    if (c.lane < 5) *(LAS unsigned*)(c.sh + L_VERD + c.lane * 4) = 0;
    u64 pc; asm volatile("s_getpc_b64 %0" : "=s"(pc));
    const char* a = (const char*)(pc & ~127ull) + (c.j * CODE_LINES + (c.lane < CODE_LINES ? c.lane : 0)) * 128;
    asm volatile("s_mov_b32 m0, %1\n\ts_nop 0\n\tglobal_load_lds_dword %0, off" :: "v"(a), "s"(c.shb + L_JUNK) : "memory");
    wait_vm<2>();
    LBAR();
  } else { BAR(); }
}
DI void late_load(const Ctx& c, const In& in, u32x4 (&v)[7]) {
  const int u = c.x * 128 + c.j * 4 + (c.w - 4);
  const bool gu = u < 640;
  u64 pg = (u64)in.wg, pu = (u64)in.wu, pd = (u64)in.wd;
  asm volatile("" : "+s"(pg), "+s"(pu), "+s"(pd));
  const rsrc_t s = __builtin_amdgcn_make_buffer_rsrc((void*)(gu ? (u >= 320 ? pu : pg) : pd), 0, 1536 * 1280, 0x00020000);
  const unsigned off = gu ? ((u >> 5) % 10) * 64 * 3072 + (u & 31) * 96 : ((u - 640) >> 3) * 32 * 1280 + (u & 7) * 160, step = gu ? 10 * 3072 : 6 * 1280;
  const unsigned r = gu ? c.lane / 6u : c.lane / 10u, ch = c.lane - r * (gu ? 6 : 10);
  const unsigned vo = c.lane < 60 ? r * (gu ? 3072 : 1280) + ch * 16 : 0x80000000u, vl = r < (gu ? 4u : 2u) ? vo : 0x80000000u;
#pragma unroll
  for (int i = 0; i < 7; ++i) v[i] = __builtin_amdgcn_raw_buffer_load_b128(s, i < 5 ? vo : i == 5 ? (gu ? vo : vl) : (gu ? vl : 0x80000000u), off + i * step, 0);
}
DI void late_store(const Ctx& c, const u32x4 (&v)[7]) {
  const int u = c.x * 128 + c.j * 4 + (c.w - 4), q = c.lane >> 4, s16 = c.lane & 15, p = c.lane & 3;
  LAS char* sh = c.sh + (c.w - 4) * STGW;
  {
    const bool gu = u < 640;
    const unsigned r = gu ? c.lane / 6u : c.lane / 10u, ch = c.lane - r * (gu ? 6 : 10);
    LAS char* d = sh + (c.lane < 60 ? r * (gu ? 112 : 176) + ch * 16 : 8176);
    const unsigned step = c.lane < 60 ? (gu ? 1120 : 1056) : 0;
    wait_vm<0>();
#pragma unroll
    for (int i = 0; i < 7; ++i) *(LAS u32x4*)(d + i * step) = v[i];
  }
  if (u < 640) {
    const int m = u / 320, kb = (u >> 5) % 10, f = (u & 31) >> 1, fh = u & 1;
    float rec = 0.f;
#pragma unroll
    for (int i = 0; i < 3; ++i) {
      const int col = i < 2 ? 8 * p + 4 * i : 32 + 4 * p, blk = i < 2 ? 2 * m + i : 4 + m;
      float cs = 0.f;
#pragma unroll
      for (int s = 0; s < 2; ++s) {
        const u32x4 e = unit_cell<112>(sh, 32 * s + 8 * q, 32 * s + 8 * q + 4, col, c.lane);
        cs = cell_sum(e, cs);
        st16(c, e, OFF_WG + f * 245760 + (2 * kb + s) * 12288 + fh * 6144 + blk * 1024 + q * 256 + s16 * 16);
      }
      cs = xadd32(xadd16(cs));
      if (q == i) rec = cs;
    }
    st4(c, rec, OFF_CSG + (((f * 2 + fh) * 2 + m) * 10 + kb) * 256 + c.lane * 4);
  } else {
    const int kk = (u - 640) >> 3, ct = u & 7;
#pragma unroll
    for (int blk = 0; blk < 5; ++blk)
      st16(c, unit_cell<176>(sh, 8 * q, 8 * q + 4, wd_colgrp(blk, p), c.lane), OFF_WD + ct * 245760 + kk * 5120 + blk * 1024 + q * 256 + s16 * 16);
  }
}
DI unsigned late_flag(const Ctx& c) {
  const int u = c.x * 128 + c.j * 4 + (c.w - 4);
  return u < 640 ? FP_G + ((u & 31) >> 1) * 64 + ((u / 320) * 10 + (u >> 5) % 10) * 2 + (u & 1) : FP_D + (u & 7) * 64 + ((u - 640) >> 3);
}

template <int P, int D, int NS, int GS, int SZ> DI void stream_pre(const Ctx& c, unsigned g, unsigned l) {
#pragma nounroll
  for (int s = 0; s < D; ++s) dma<P>(c, g + s * GS, l + s * SZ);
}
template <int P, int E, int K, int K1> DI void stream_tail(const Ctx& c, int ls = 0) {
  if constexpr (K >= K1) { wait_vm<K * P + E>(); BAR(); stream_tail<P, E, K - 1, K1>(c, ls + 1); }
}
template <int P, int D, int NS, int GS, int SZ, int K0>
DI void stream_loop(const Ctx& c, unsigned g, unsigned l, int ls = 0) {
  unsigned slot = ((K0 - 1) % D) * SZ; g += (D + K0 - 1) * GS;
#pragma nounroll
  for (int k = K0; k <= NS - D; ++k) {
    wait_vm<(D - 2) * P>();
    BAR();
    dma<P>(c, g, l + slot);
    g += GS; slot = slot == (D - 1) * SZ ? 0 : slot + SZ;
  }
}
template <int P, int D, int NS, int GS, int SZ, int K, int HI>
DI void stream_ramp(const Ctx& c, unsigned g, unsigned l, int ls) {
  if constexpr (K >= 1 && HI == K + D - 2) stream_loop<P, D, NS, GS, SZ, K>(c, g, l, ls);
  else {
    if (K > 0) wait_vm<(HI - K) * P>();
    BAR();
    constexpr int CAP = K == 0 ? D - 1 : K - 1 + D, H2 = HI + 2 < CAP ? HI + 2 : CAP;
#pragma nounroll
    for (int s = HI + 1; s <= H2; ++s) dma<P>(c, g + s * GS, l + (s % D) * SZ);
    stream_ramp<P, D, NS, GS, SZ, K + 1, H2>(c, g, l, ls);
  }
}
template <int P, int D, int NS, int GS, int SZ, int E, int EY, class F1, class F2>
DI void stream_acts(const Ctx& c, unsigned g, unsigned l, int ls, F1&& extra, F2&& finish) {
  constexpr int GH = 81920;
  {
    const int p = c.w - 4;
    const unsigned g0 = g - p * GH + p * GS, l0 = l - p * (SZ / 2) + p * SZ;
    dma<P>(c, g0, l0); dma<P>(c, g0 + GH, l0 + SZ / 2);
    if (p == 0) wait_vm<0>();
  }
  stream_ramp<P, D, NS, GS, SZ, 0, 1>(c, g, l, ls);
  extra();
  stream_tail<P, E, D - 2, 1>(c, ls + 3 + NS - D);
  finish();
  wait_vm<EY>();
  BAR();
  BAR();
}
template <int N> DI void wbar() { wait_vm<N>(); BAR(); }
struct Stats { u32x2 v[8]; };
DI void stats_load(const Ctx& c, Stats& s, unsigned off, int row) {
#pragma unroll
  for (int k = 0; k < 8; ++k) s.v[k] = ld8(c, (row >> 4) * 1024 + (row & 15) * 8 + k * 128, off);
}
DI void stats_finish(const Ctx& c, Stats& s, int t) {
  float sum = 0.f, sq = 0.f;
#pragma unroll
  for (int k = 0; k < 8; ++k) PIN(s.v[k]);
#pragma unroll
  for (int k = 0; k < 8; ++k) { sum += u2f(s.v[k][0]); sq += u2f(s.v[k][1]); }
  const float mean = sum * (1.f / 640.f), var = fmaxf(sq * (1.f / 640.f) - mean * mean, 0.f);
  *(LAS float*)(c.sh + L_MU + t * 4) = mean;
  *(LAS float*)(c.sh + L_RS + t * 4) = __builtin_amdgcn_rsqf(var + 1e-5f);
  asm volatile("s_waitcnt lgkmcnt(0)" ::: "memory");
}
struct Cs { unsigned v[10]; };
DI void cs_load(const Ctx& c, Cs& s, unsigned off, unsigned rec, int idx) {
#pragma unroll
  for (int k = 0; k < 10; ++k) s.v[k] = __builtin_amdgcn_raw_buffer_load_b32(c.srd, rec * 2560 + idx * 4 + k * 256, off, SC1);
}
DI float cs_sum(Cs& s) {
#pragma unroll
  for (int k = 0; k < 10; ++k) PIN(s.v[k]);
  float a = 0.f;
#pragma unroll
  for (int k = 0; k < 10; ++k) a += u2f(s.v[k]);
  return a;
}

DI u32x4 ln_cell(f32x4 a, f32x4 b, f32x4 ca, f32x4 cb, float mu, float rs) {
  return u32x4{pk2((lo2(a) - lo2(ca) * mu) * rs), pk2((hi2(a) - hi2(ca) * mu) * rs), pk2((lo2(b) - lo2(cb) * mu) * rs), pk2((hi2(b) - hi2(cb) * mu) * rs)};
}
DI f32x4 q4(const f32x16& a, int i) { return f32x4{a[4 * i], a[4 * i + 1], a[4 * i + 2], a[4 * i + 3]}; }

DI void qkv_consumer(const Ctx& c, int L) {
  const int i = row16(c), t = col16(c);
  const int th = c.w >> 1, fh = c.w & 1, q = c.lane >> 4, s16 = c.lane & 15;
  f32x16 a0 = {}, a1 = {}, a2 = {}, a3 = {};
  const unsigned aA = c.shb + th * 8192 + q * 1024 + s16 * 16, aB = c.shb + 65536 + fh * 4096 + c.lane * 16;
  const unsigned oq = OFF_Q + L * LS_Q + (i * 4 + th * 2) * QTL, okv = OFF_KV + L * LS_KV + (i * 4 + th * 2) * KVT;
  const unsigned aM = c.shb + L_MU + (th * 64 + s16) * 4, aC = c.shb + L_CSQ + q * 16;
  f32x2 m01, r01, m23, r23; unsigned cnt, sa, sb;
  if (fh == 0) {
    unsigned vq = oq + (2 * t + (q >> 1)) * (64 * QTL) + (q & 1) * 512 + s16 * 16, vk = okv + (2 * t + (q >> 1)) * (64 * KVT) + (q & 1) * 512 + s16 * 16;
    PIN(vq); PIN(vk);
    f32x4 cs[4];
    asm volatile(QKV0_ASM : "+{v[0:15]}"(a0), "+{v[16:31]}"(a1), "+{v[32:47]}"(a2), "+{v[48:63]}"(a3),
                            QKV0_O0(m01), QKV0_O1(r01), QKV0_O2(cs[2]), QKV0_O3(cs[3]), QKV0_O4(cs[0]), QKV0_O5(cs[1]), QKV0_O6(m23), QKV0_O7(r23), [cnt] "=&s"(cnt), [sa] "=&s"(sa), [sb] "=&s"(sb)
                 : QKV0_AA(aA), QKV0_AB(aB), QKV0_AT0(aM), QKV0_AT1(aC) : QKV0_CLOB, "scc", "memory");
#pragma unroll
    for (int tb = 0; tb < 4; ++tb) {
      const f32x16& a = tb == 0 ? a0 : tb == 1 ? a1 : tb == 2 ? a2 : a3;
      const float mu = (tb < 2 ? m01 : m23)[tb & 1], rs = (tb < 2 ? r01 : r23)[tb & 1];
      st16(c, ln_cell(q4(a, 2), q4(a, 3), cs[2], cs[3], mu, rs), vk + (tb >> 1) * KVT + (tb & 1) * 256);
      st16(c, ln_cell(q4(a, 0), q4(a, 1), cs[0], cs[1], mu, rs * QSCALE), vq + (tb >> 1) * QTL + (tb & 1) * 256);
    }
  } else {
    unsigned vt = (q < 2 ? oq + (2 * t + (q & 1)) * (64 * QTL) : okv + (2 * t + (q & 1)) * (64 * KVT)) + 1024 + s16 * 16, vts = q < 2 ? QTL : KVT;
    unsigned vv[3];
#pragma unroll
    for (int b = 0; b < 3; ++b) {
      const int sg = 16 * b + s16, hb = sg < 40 ? (sg >= 20) : (sg & 1), d = sg < 40 ? sg - 20 * hb : 20 + ((sg >> 1) & 1);
      vv[b] = sg < ((REUSE & 2) && L ? 40 : 44) ? okv + (2 * t + hb) * (64 * KVT) + 1280 + d * 64 + ((q ^ ((d >> 2) & 3)) * 16) : 0x80000000u;
      PIN(vv[b]);
    }
    unsigned pad = s16 < 10 ? 0x3f803f80u : 0u; float qs = q < 2 ? QSCALE : 1.f;
    PIN(vt); PIN(vts); PIN(pad); PIN(qs);
    f32x4 cst, mv[2][2], rv[2][2]; f32x2 cv01; float cv2;
    asm volatile(QKV1_ASM : "+{v[0:15]}"(a0), "+{v[16:31]}"(a1), "+{v[32:47]}"(a2), "+{v[48:63]}"(a3),
                            QKV1_O0(mv[0][0]), QKV1_O1(mv[0][1]), QKV1_O2(rv[0][0]), QKV1_O3(rv[0][1]), QKV1_O4(cv01), QKV1_O5(cv2), QKV1_O6(cst), QKV1_O7(m01), QKV1_O8(r01),
                            QKV1_O9(mv[1][0]), QKV1_O10(mv[1][1]), QKV1_O11(rv[1][0]), QKV1_O12(rv[1][1]), QKV1_O13(m23), QKV1_O14(r23), [cnt] "=&s"(cnt), [sa] "=&s"(sa), [sb] "=&s"(sb)
                 : QKV1_AA(aA), QKV1_AB(aB), QKV1_AT0(aM), QKV1_AT1(aC), QKV1_AT2(c.shb + L_MU + (th * 64 + q * 4) * 4), QKV1_AT3(c.shb + L_CSQ + (80 + s16) * 4)
                 : QKV1_CLOB, "scc", "memory");
    const float csv[3] = {cv01[0], cv01[1], cv2};
#pragma unroll
    for (int tp = 0; tp < 2; ++tp) {
      const f32x16& aa = tp == 0 ? a0 : a2; const f32x16& ab = tp == 0 ? a1 : a3;
      const f32x4 mua = mv[tp][0], mub = mv[tp][1], rsa = rv[tp][0], rsb = rv[tp][1];
#pragma unroll
      for (int b = 0; b < 3; ++b) {
        const f32x4 x = q4(aa, 1 + b), y = q4(ab, 1 + b);
        u32x4 pv = {pk2((lo2(x) - lo2(mua) * csv[b]) * lo2(rsa)), pk2((hi2(x) - hi2(mua) * csv[b]) * hi2(rsa)),
                    pk2((lo2(y) - lo2(mub) * csv[b]) * lo2(rsb)), pk2((hi2(y) - hi2(mub) * csv[b]) * hi2(rsb))};
        if (b == 2 && s16 >= 8) pv = u32x4{pad, pad, pad, pad};
        st16(c, pv, vv[b] + tp * KVT);
      }
      const float m0 = (tp ? m23 : m01)[0], m1 = (tp ? m23 : m01)[1], r0 = (tp ? r23 : r01)[0] * qs, r1 = (tp ? r23 : r01)[1] * qs;
      const f32x4 x = q4(aa, 0), y = q4(ab, 0);
      st16(c, u32x4{pk2((lo2(x) - lo2(cst) * m0) * r0), pk2((hi2(x) - hi2(cst) * m0) * r0), pk2((lo2(y) - lo2(cst) * m1) * r1), pk2((hi2(y) - hi2(cst) * m1) * r1)}, vt + tp * vts);
    }
  }
  stores_issued(c);
  arrive(c, L * 24 + 4, 16, MB_Q + L * MB_LAYER, t * 16 + c.lane);
}
template <bool FIRST> DI void qkv_streamer(const Ctx& c, int L, const In& in) {
  u32x4 lv[7];
  const int i = row16(c), t = col16(c);
  if (c.w < 6) {
    if (FIRST) await_inputs<6>(c, 0, MB_X, i * 16 + t, 16); else await_inputs(c, 24, MB_D, i * 16 + t, 32);
    Stats s; Cs cs, cs2;
    const int sl = (c.w - 4) * 64 + c.lane, gb = sl >> 4, s16 = sl & 15;
    stream_acts<8, 4, 10, 8192, 16384, FIRST ? 35 : 8, FIRST ? 7 : 0>(c, OFF_X + L * LS_ACT + (2 * i + c.w - 4) * 81920, c.shb + (c.w - 4) * 8192, FIRST ? 200 : 0,
      [&] { stats_load(c, s, OFF_SX + L * LS_ST, i * 128 + sl);
            if (FIRST) {
              const int rc = (t * 3 + (gb < 4 ? gb >> 1 : gb == 4 ? s16 >> 3 : 2)) * 2, ix = gb < 4 ? (gb & 1) * 16 + s16 : gb == 4 ? 32 + (s16 & 7) : (gb - 5) * 16 + s16;
              cs_load(c, cs, OFF_CSQ, rc, ix); cs_load(c, cs2, OFF_CSQ, rc + 1, ix);
            }
            if (FIRST) late_load(c, in, lv); },
      [&] { if (FIRST) *(LAS float*)(c.sh + L_CSQ + sl * 4) = cs_sum(cs) + cs_sum(cs2);
            stats_finish(c, s, sl); });
    if (FIRST) late_store(c, lv);
  } else {
    const int p = c.w - 6;
    const unsigned g = OFF_WQ + t * 163840 + p * 8192, l = c.shb + 65536 + p * 8192;
    if (FIRST) {
      if (p == 0) { wait_flags<6>(c, mb_word(MB_WQ, i * 16 + t, 0), 16); lds_set(c, L_GO2, 1); }
      else lds_wait(c, L_GO2, 1);
      dma<16>(c, g + p * 8192, l + p * 8192);
      if (p == 0) wait_vm<0>();
      stream_ramp<8, 5, 10, 16384, 16384, 0, 1>(c, g, l, 300);
    } else {
      wbar<0>();
      stream_loop<8, 5, 10, 16384, 16384, 1>(c, g, l);
    }
    wbar<24>(); wbar<16>();
    if (FIRST) {
      late_load(c, in, lv);
      asm volatile("s_mov_b32 m0, %1\n\ts_nop 0\n\tglobal_load_lds_dword %0, off" :: "v"(in.out + blockIdx.x * 10240), "s"(c.shb + L_JUNK) : "memory");
    }
    wbar<FIRST ? 16 : 8>(); wbar<FIRST ? 8 : 0>();
    BAR();
    if (FIRST) { late_store(c, lv); wait_flags(c, FP_O + col8(c) * 64, 16); }
  }
}

DI void gu_pre(const Ctx& c, int s) {
  const unsigned o = (c.w - 6) * 12288 + s * 24576;
  stream_pre<12, 2, 10, 24576, 24576>(c, OFF_WG + col16(c) * 245760 + o, c.shb + o);
}

DI f32x16 qk_tile(u32x4 km, u32x2 kt, u32x4 qm, u32x2 qt) {
  f32x16 s = {};
  s = __builtin_amdgcn_mfma_f32_32x32x8bf16_1k(__builtin_bit_cast(s16x4, kt), __builtin_bit_cast(s16x4, qt), s, 0, 0, 0);
  return __builtin_amdgcn_mfma_f32_32x32x16_bf16(__builtin_bit_cast(bf16x8, km), __builtin_bit_cast(bf16x8, qm), s, 0, 0, 0);
}
DI void attention(const Ctx& c, int L) {
  const int hd = 4 * c.x + (c.j >> 3), qp = c.j & 7, g = c.w >> 2, wq = c.w & 3;
  const int r = c.lane & 31, h = c.lane >> 5;
  const unsigned kv0 = OFF_KV + L * LS_KV + hd * (64 * KVT);
  const unsigned qb = OFF_Q + L * LS_Q + hd * (64 * QTL) + (qp * 8 + wq * 2) * QTL, q0 = qb + g * QTL, q1 = qb + (g ^ 1) * QTL;
  unsigned vD = c.w * 1344 + c.lane * 16; const unsigned lD = c.shb + L_RING + c.w * 1344;
  const int tau = 16 * ((r >> 2) & 1) + 4 * (r >> 3) + (r & 3);
  const unsigned cKm = h * 512 + tau * 16, cKt = h ? 1280 + 20 * 64 : 1024 + (tau & 15) * 16 + (tau >> 4) * 8;
  const unsigned rb = c.shb + L_RING + g * (2 * KVT);
  unsigned aKm = rb + cKm, aKt = rb + cKt, aV0 = rb + 1280 + (r < 21 ? r : 21) * 64 + ((h ^ (r < 21 ? (r >> 2) & 3 : r < 28 ? 1 : 3)) * 16), aV1 = aV0 ^ 32;
  unsigned aXm = c.shb + L_RING + cKm, aXt = c.shb + L_RING + cKt;
  unsigned vL = c.lane * 16, vQt = h ? 0x80000000u : 1024 + (r & 15) * 16 + (r >> 4) * 8;
  unsigned vo = OFF_O + L * LS_O + (qp * 4 + wq) * 81920 + (5 * (hd >> 1)) * 1024 + (g * 32 + r) * 16;
  unsigned vo0 = vo + (2 * (hd & 1) + h) * 1024, vo1 = h ? 0x80000000u : vo + 4096 + (hd & 1) * 8;
  unsigned vF = late_flag(c) * 8, xw = c.shb + c.w * 3072 + c.lane * 16, xr = c.shb + (c.w ^ 4) * 3072 + c.lane * 16;
  u32x2 ep = {(unsigned)c.ep, (unsigned)(c.ep >> 32)};
  const unsigned wp0 = (c.w < 7 ? c.w * 13 : 87) * 1024;
  f32x16 oA = {}, oB = {};
  float nA = 0.f, nB = 0.f;
  asm volatile("" : "+{v[64:79]}"(oA), "+{v[80:95]}"(oB), "+{v96}"(vL), "+{v97}"(vQt), "+{v98}"(aXm), "+{v99}"(aXt), "+{v100}"(nA), "+{v101}"(nB), "+{v[102:103]}"(ep), "+{v104}"(vF),
                    "+{v169}"(aKm), "+{v170}"(aKt), "+{v171}"(aV0), "+{v172}"(aV1), "+{v173}"(vD));
  PIN(vo0); PIN(vo1); PIN(xw); PIN(xr);
  if (c.w == 4) await_inputs(c, L * 24 + 4, MB_Q + L * MB_LAYER, hd * 8 + qp, 16);
  LBAR();
  f32x4 e0, e1, e2;
  auto pass = [&](unsigned again) -> bool {
    unsigned so = kv0, trip, wsrc = OFF_WO + col8(c) * 102400 + wp0, wl = c.shb + L_WB + wp0;
    asm volatile(ATTN_ASM : "+{v[64:79]}"(oA), "+{v[80:95]}"(oB), "+{v100}"(nA), "+{v101}"(nB), [so] "+s"(so), [ws] "+s"(wsrc), [wl] "+s"(wl), [trip] "=&s"(trip)
                 : "{v96}"(vL), "{v97}"(vQt), "{v98}"(aXm), "{v99}"(aXt), "{v[102:103]}"(ep), "{v104}"(vF),
                   "{v169}"(aKm), "{v170}"(aKt), "{v171}"(aV0), "{v172}"(aV1), "{v173}"(vD),
                   [srd] "s"(c.srd), [lds] "s"(lD), [junk] "s"(c.shb + L_JUNK), [q0] "s"(q0), [q1] "s"(q1), [again] "s"(again),
                   [late] "s"((unsigned)(g & (L ^ 1)))
                 : ATTN_CLOB, "vcc", "scc", "memory");
    LAS char* pw = (LAS char*)(uintptr_t)xw; LAS char* pr = (LAS char*)(uintptr_t)xr;
    *(LAS f32x4*)(pw) = q4(oB, 0); *(LAS f32x4*)(pw + 1024) = q4(oB, 1); *(LAS f32x4*)(pw + 2048) = q4(oB, 2);
    f32x2 t0 = lo2(q4(oA, 0)) * 1024.f, t1 = hi2(q4(oA, 0)) * 1024.f, t2 = lo2(q4(oB, 0)) * 1024.f, t3 = hi2(q4(oB, 0)) * 1024.f;
#pragma unroll
    for (int e = 1; e < 3; ++e) {
      t0 = __builtin_elementwise_fma(lo2(q4(oA, e)), f32x2{1024.f, 1024.f}, t0); t1 = __builtin_elementwise_fma(hi2(q4(oA, e)), f32x2{1024.f, 1024.f}, t1);
      t2 = __builtin_elementwise_fma(lo2(q4(oB, e)), f32x2{1024.f, 1024.f}, t2); t3 = __builtin_elementwise_fma(hi2(q4(oB, e)), f32x2{1024.f, 1024.f}, t3);
    }
    t0 = (t0 * 0.f + t1 * 0.f) + (t2 * 0.f + t3 * 0.f);
    if (__any(!(t0[0] + t0[1] == 0.f))) lds_set(c, L_VERD, L + 1);
    LBAR();
    unsigned vd = *(LAS unsigned*)(c.sh + L_VERD);
    e0 = *(LAS f32x4*)(pr); e1 = *(LAS f32x4*)(pr + 1024); e2 = *(LAS f32x4*)(pr + 2048);
    PIN(vd); PIN(e0); PIN(e1); PIN(e2);
    e0 += q4(oA, 0); e1 += q4(oA, 1); e2 += q4(oA, 2);
    return (unsigned)__builtin_amdgcn_readfirstlane(vd) == (unsigned)L + 1;
  };
  if (__builtin_expect(pass(0), 0)) {
    const u32x4 qAm = ld16(c, vL, q0), qBm = ld16(c, vL, q1); const u32x2 qAt = ld8(c, vQt, q0), qBt = ld8(c, vQt, q1);
    float mA = -INFINITY, mB = -INFINITY;
#pragma nounroll
    for (int n = 0; n < 64; ++n) {
      const u32x4 km = ld16(c, cKm, kv0 + n * KVT); const u32x2 kt = ld8(c, cKt, kv0 + n * KVT);
      const f32x16 sA = qk_tile(km, kt, qAm, qAt), sB = qk_tile(km, kt, qBm, qBt);
#pragma unroll
      for (int e = 0; e < 16; ++e) { mA = fmaxf(mA, sA[e]); mB = fmaxf(mB, sB[e]); }
    }
    nA = -xmax32(mA); nB = -xmax32(mB);
    oA = f32x16{}; oB = f32x16{};
    LBAR();
    pass(1);
  }
  const auto lr = __builtin_amdgcn_permlane32_swap(f2u(e2[0]), f2u(e2[0]), false, false);
  const float inv = __builtin_amdgcn_rcpf(u2f(lr[1]));
  st16(c, u32x4{pk2(lo2(e0) * inv), pk2(hi2(e0) * inv), pk2(lo2(e1) * inv), pk2(hi2(e1) * inv)}, vo0);
  st8(c, u32x2{pk2(lo2(e2) * inv), pk2(hi2(e2) * inv)}, vo1);
  stores_issued(c);
}
template <int YOUNGER> DI void attention_done(const Ctx& c, int L) {
  const bool kv = c.lane >= 32;
  arrive<YOUNGER>(c, L * 24 + 12, L == 0 ? 48 : 32, kv ? MB_D : MB_A + L * MB_LAYER, kv ? (c.lane - 32) * 16 + 2 * c.x + (c.j >> 4) : (c.j & 7) * 32 + c.lane);
}

template <bool DOWN> DI void wd_consumer(const Ctx& c, int L, char* out) {
  const int r = row32(c), ct = col8(c), th = c.w >> 1, kh = c.w & 1, q = c.lane >> 4, s16 = c.lane & 15;
  const int tok = th * 32 + kh * 16 + s16;
  const unsigned cell = r * 81920 + ct * 10240 + tok * 16;
  const unsigned ores = DOWN ? OFF_X2 + L * LS_ACT : OFF_X + L * LS_ACT;
  const u32x4 ra = ld16(c, cell + q * 1024, ores), rb = ld16(c, cell + (4 + q) * 1024, ores);
  const u32x2 rc = ld8(c, cell + (8 + (q >> 1)) * 1024 + (q & 1) * 8, ores);
  f32x4 k0 = {bflo(ra[0]), bfhi(ra[0]), bflo(ra[1]), bfhi(ra[1])}, k1 = {bflo(ra[2]), bfhi(ra[2]), bflo(ra[3]), bfhi(ra[3])};
  f32x4 k2 = {bflo(rb[0]), bfhi(rb[0]), bflo(rb[1]), bfhi(rb[1])}, k3 = {bflo(rb[2]), bfhi(rb[2]), bflo(rb[3]), bfhi(rb[3])};
  f32x4 k4 = {bflo(rc[0]), bfhi(rc[0]), bflo(rc[1]), bfhi(rc[1])};
  f32x4 s0 = {}, s1 = {}, s2 = {}, s3 = {}, s4 = {};
  const unsigned aX = (kh * 4 + q) * 1024 + tok * 16, aB = c.shb + kh * 5120 + c.lane * 16;
  unsigned so = DOWN ? OFF_H + L * LS_H + r * 196608 : OFF_O + L * LS_O + r * 81920;
  const bool last = DOWN && L == 1;
  const unsigned od = DOWN ? OFF_X + LS_ACT : OFF_X2 + L * LS_ACT, os = DOWN ? OFF_SX + LS_ST : OFF_S2 + L * LS_ST;
  unsigned va = od + cell + q * 1024, vc = (q & 1) ? 0x80000000u : od + cell + (8 + (q >> 1)) * 1024, vs = q ? 0x80000000u : os + ((r * 64 + tok) >> 4) * 1024 + ct * 128 + s16 * 8;
  unsigned xw = c.shb + (DOWN ? 0 : 102400) + c.w * 5120 + c.lane * 16, xr = c.shb + (DOWN ? 0 : 102400) + (c.w ^ 1) * 5120 + c.lane * 16;
  PIN(va); PIN(vc); PIN(vs);
#define WD_OUT "+{v[0:3]}"(k0), "+{v[4:7]}"(k1), "+{v[8:11]}"(k2), "+{v[12:15]}"(k3), "+{v[16:19]}"(k4), \
               "+{v[20:23]}"(s0), "+{v[24:27]}"(s1), "+{v[28:31]}"(s2), "+{v[32:35]}"(s3), "+{v[36:39]}"(s4), [so] "+s"(so)
  if (DOWN) asm volatile(DN_ASM : WD_OUT : DN_AX0(aX), DN_AX1(aX ^ 256), DN_AB(aB), DN_AE0(xw), DN_AE1(xr), [srd] "s"(c.srd) : DN_CLOB, "scc", "memory");
  else      asm volatile(WO_ASM : WD_OUT : WO_AX0(aX), WO_AX1(aX ^ 256), WO_AB(aB), WO_AE0(xw), WO_AE1(xr), [srd] "s"(c.srd) : WO_CLOB, "scc", "memory");
  const u32x4 ca = {pk2(lo2(k0)), pk2(hi2(k0)), pk2(lo2(k1)), pk2(hi2(k1))}, cb = {pk2(lo2(k2)), pk2(hi2(k2)), pk2(lo2(k3)), pk2(hi2(k3))};
  const unsigned p8 = pk2(lo2(k4)), p9 = pk2(hi2(k4));
  const auto w0 = __builtin_amdgcn_permlane16_swap(p8, p8, false, false), w1 = __builtin_amdgcn_permlane16_swap(p9, p9, false, false);
  const u32x4 cc = {p8, p9, w0[1], w1[1]};
  if (last) {
    GAS char* o = (GAS char*)out + (r * 64 + tok) * 1280 + ct * 160;
    *(GAS u32x4*)(o + q * 16) = ca; *(GAS u32x4*)(o + 64 + q * 16) = cb;
    if (!(q & 1)) *(GAS u32x4*)(o + 128 + (q >> 1) * 16) = cc;
  } else {
    st16(c, ca, va); st16(c, cb, va + 4096); st16(c, cc, vc);
    const f32x2 su = ((lo2(k0) + hi2(k0)) + (lo2(k1) + hi2(k1))) + ((lo2(k2) + hi2(k2)) + (lo2(k3) + hi2(k3))) + (lo2(k4) + hi2(k4));
    f32x2 qa = lo2(k0) * lo2(k0), qb = hi2(k0) * hi2(k0);
    qa += lo2(k1) * lo2(k1); qb += hi2(k1) * hi2(k1); qa += lo2(k2) * lo2(k2); qb += hi2(k2) * hi2(k2);
    qa += lo2(k3) * lo2(k3); qb += hi2(k3) * hi2(k3); qa += lo2(k4) * lo2(k4); qb += hi2(k4) * hi2(k4);
    qa += qb;
    const auto e16 = __builtin_amdgcn_permlane16_swap(f2u(su[0] + su[1]), f2u(qa[0] + qa[1]), false, false);
    const float hs = xadd32(u2f(e16[0]) + u2f(e16[1]));
    const auto f16 = __builtin_amdgcn_permlane16_swap(f2u(hs), f2u(hs), false, false);
    st8(c, u32x2{f16[0], f16[1]}, vs);
    stores_issued(c);
    arrive(c, L * 24 + (DOWN ? 24 : 16), 16, (DOWN ? MB_D : MB_W) + L * MB_LAYER, (r >> 1) * 16 + c.lane);
  }
}
template <int A, int B> DI void park_load(const Ctx& c, u32x4 (&park)[45]) {
#pragma unroll
  for (int k = A; k < B; ++k) park[k] = ld16(c, c.lane * 16 + (k % 5) * 1024, OFF_WD + col8(c) * 245760 + (c.w - 6) * 5120 + (15 + k / 5) * 10240);
}
template <bool DOWN, bool WTS> DI void wd_streamer(const Ctx& c, int L, u32x4 (&park)[45]) {
  const int r = row32(c), ct = col8(c), p = c.w - 6;
  if (!WTS) {
    if (!DOWN) attention_done<0>(c, L);
    if (c.w == 4) {
      if (DOWN) await_inputs(c, L * 24 + 20, MB_G + L * MB_LAYER, r * 8 + ct, 16); else await_inputs(c, L * 24 + 12, MB_A + L * MB_LAYER, r * 8 + ct, 32);
    }
    BAR();
    if (!DOWN && L == 0) {
      __builtin_amdgcn_s_sleep(10);
      Cs c0, c1;
      const int f = col16(c), pa = c.w - 4, bq = c.lane >> 4, s16 = c.lane & 15;
      cs_load(c, c0, OFF_CSG, (f * 2 + pa) * 2 + (bq >> 1), (bq & 1) * 16 + s16); cs_load(c, c1, OFF_CSG, (f * 2 + pa) * 2 + (bq & 1), 32 + s16);
      *(LAS float*)(c.sh + L_CSG + (pa * 96 + c.lane) * 4) = cs_sum(c0); *(LAS float*)(c.sh + L_CSG + (pa * 96 + 64 + (c.lane & 31)) * 4) = cs_sum(c1);
    }
    LBAR(); BAR();
    if (DOWN) { BAR(); BAR(); BAR(); BAR(); }
  } else if (!DOWN) {
    const int f = col16(c);
    if (L == 0) {
      GAS u64* fg = (GAS u64*)c.ws + FP_G + f * 64 + (c.lane < 40 ? c.lane : 0); GAS u64* fd = (GAS u64*)c.ws + FP_D + ct * 64 + (c.lane < 48 ? c.lane : 0);
      const u64 a = __hip_atomic_load(fg, RLX_AGENT), b = __hip_atomic_load(fd, RLX_AGENT);
      attention_done<0>(c, L);
      if (!__all(a == c.ep && b == c.ep)) { wait_flags(c, FP_G + f * 64, 40); wait_flags(c, FP_D + ct * 64, 48); }
      lds_wait(c, L_CNT2, L * 24 + 12);
      gu_pre(c, 0);
      park_load<0, 24>(c, park);
      BAR(); BAR(); BAR();
    } else {
      lds_wait(c, L_CNT2, L * 24 + 12);
      gu_pre(c, 0);
      attention_done<24>(c, L);
      park_load<0, 24>(c, park);
      BAR(); BAR(); BAR();
    }
    lds_wait(c, L_CNT2, L * 24 + 16);
    gu_pre(c, 2);
    park_load<24, 32>(c, park);
  } else {
    const int t = col16(c);
    LAS char* l = c.sh + 7168 + p * 5120 + c.lane * 16;
    auto put = [&](int s) {
#pragma unroll
      for (int k = 0; k < 5; ++k) { *(LAS u32x4*)(l + DN_SLOT[s] * 10240 + k * 1024) = park[s * 5 + k]; }
    };
    wbar<0>();
    BAR(); put(0); put(1); put(2);
    BAR(); put(3); put(4); put(5);
    LBAR();
    const unsigned gq = OFF_WQ + t * 163840 + p * 8192, lq = c.shb + 65536 + p * 8192;
    if (L == 0) dma<8>(c, gq + 2 * 16384, lq + 2 * 16384);
    put(6); put(7); put(8);
    LBAR();
    BAR(); BAR();
    if (L == 0) { lds_wait(c, L_CNT2, 24); stream_pre<8, 2, 10, 16384, 16384>(c, gq, lq); stream_pre<8, 2, 10, 16384, 16384>(c, gq + 3 * 16384, lq + 3 * 16384); }
  }
}

DI unsigned reglu2(f32x2 g, f32x2 u, f32x2 cg, f32x2 cu, float mu, float r2) {
  g -= cg * mu; u = (u - cu * mu) * r2;
  return pk2(f32x2{fmaxf(g[0], 0.f), fmaxf(g[1], 0.f)} * u);
}
DI void gu_consumer(const Ctx& c, int L) {
  const int i = row16(c), f = col16(c);
  const int th = c.w >> 1, fh = c.w & 1, q = c.lane >> 4, s16 = c.lane & 15;
  f32x16 a0 = {}, a1 = {}, a2 = {}, a3 = {}, a4 = {}, a5 = {};
  const unsigned aA = c.shb + 98304 + th * 8192 + q * 1024 + s16 * 16, aB = c.shb + fh * 6144 + c.lane * 16;
  const unsigned oh = OFF_H + L * LS_H + (2 * i + th) * 196608 + (f * 12 + fh * 6) * 1024 + s16 * 16;
  unsigned va = oh + q * 1024, vz = oh + (4 + (q >> 1)) * 1024 + (q & 1) * 256;
  PIN(va); PIN(vz);
  f32x4 cs[6]; f32x2 m01, r01, m23, r23; unsigned cnt, sa, sb;
  asm volatile(GU_ASM : "+{v[0:15]}"(a0), "+{v[16:31]}"(a1), "+{v[32:47]}"(a2), "+{v[48:63]}"(a3), "+{v[64:79]}"(a4), "+{v[80:95]}"(a5),
                        GU_O0(m01), GU_O1(r01), GU_O2(cs[0]), GU_O3(cs[1]), GU_O4(cs[2]), GU_O5(cs[3]), GU_O6(cs[4]), GU_O7(cs[5]), GU_O8(m23), GU_O9(r23), [cnt] "=&s"(cnt), [sa] "=&s"(sa), [sb] "=&s"(sb)
               : GU_AA(aA), GU_AB(aB), GU_AT0(c.shb + L_MU + (th * 64 + s16) * 4), GU_AT1(c.shb + L_CSG + (fh * 96 + q * 4) * 4) : GU_CLOB, "scc", "memory");
  auto acc = [&](int k) -> f32x4 { return q4(k < 4 ? a0 : k < 8 ? a1 : k < 12 ? a2 : k < 16 ? a3 : k < 20 ? a4 : a5, k & 3); };
#pragma unroll
  for (int tp = 0; tp < 2; ++tp) {
    unsigned z[2][2];
#pragma unroll
    for (int e = 0; e < 2; ++e) {
      const int tb = 2 * tp + e;
      const float mu = (tp ? m23 : m01)[e], rs = (tp ? r23 : r01)[e], r2 = rs * rs;
      const f32x4 xg = acc(tb * 6), yg = acc(tb * 6 + 1), xu = acc(tb * 6 + 2), yu = acc(tb * 6 + 3), zg = acc(tb * 6 + 4), zu = acc(tb * 6 + 5);
      st16(c, u32x4{reglu2(lo2(xg), lo2(xu), lo2(cs[0]), lo2(cs[2]), mu, r2), reglu2(hi2(xg), hi2(xu), hi2(cs[0]), hi2(cs[2]), mu, r2),
                    reglu2(lo2(yg), lo2(yu), lo2(cs[1]), lo2(cs[3]), mu, r2), reglu2(hi2(yg), hi2(yu), hi2(cs[1]), hi2(cs[3]), mu, r2)}, va + tb * 256);
      z[e][0] = reglu2(lo2(zg), lo2(zu), lo2(cs[4]), lo2(cs[5]), mu, r2); z[e][1] = reglu2(hi2(zg), hi2(zu), hi2(cs[4]), hi2(cs[5]), mu, r2);
    }
    const auto w0 = __builtin_amdgcn_permlane16_swap(z[0][0], z[1][0], false, false), w1 = __builtin_amdgcn_permlane16_swap(z[0][1], z[1][1], false, false);
    st16(c, u32x4{w0[0], w1[0], w0[1], w1[1]}, vz + tp * 512);
  }
  stores_issued(c);
  arrive(c, L * 24 + 20, 16, MB_G + L * MB_LAYER, i * 16 + c.lane);
}
template <bool WTS> DI void gu_streamer(const Ctx& c, int L, u32x4 (&park)[45]) {
  const int i = row16(c), f = col16(c);
  if (!WTS) {
    const int p = c.w - 4;
    await_inputs(c, L * 24 + 16, MB_W + L * MB_LAYER, i * 16 + f, 16);
    Stats s;
    stream_acts<8, 3, 10, 8192, 16384, 8, 0>(c, OFF_X2 + L * LS_ACT + (2 * i + p) * 81920, c.shb + 98304 + p * 8192, 32,
      [&] { stats_load(c, s, OFF_S2 + L * LS_ST, i * 128 + p * 64 + c.lane); }, [&] { stats_finish(c, s, p * 64 + c.lane); });
  } else {
    const int p = c.w - 6;
    const unsigned gg = OFF_WG + f * 245760 + p * 12288, lg = c.shb + p * 12288;
    wbar<8>();
    stream_loop<12, 4, 10, 24576, 24576, 1>(c, gg, lg);
    const unsigned g = OFF_WD + col8(c) * 245760 + p * 5120, l = c.shb + 7168 + p * 5120;
    auto wd = [&](int s) { dma<5>(c, g + s * 10240, l + DN_SLOT[s] * 10240); };
    wbar<24>();
    wd(0); wd(1); wbar<22>();
    wd(2); wd(3); wd(4); park_load<32, 38>(c, park); wbar<31>();
    wd(5); wd(6); wd(7); park_load<38, 45>(c, park); BAR();
    lds_wait(c, L_CNT2, L * 24 + 20);
    wd(8); wd(9); wd(10); wd(11); wd(12); wd(13); wd(14);
  }
}

__global__ void __launch_bounds__(512, 2) fused_kernel(const char* x, const char* wqkv, const char* wo, const char* wg, const char* wu, const char* wd, char* ws, char* out) {
  extern __shared__ __attribute__((aligned(16))) char shm[];
  Ctx c;
  c.srd = __builtin_amdgcn_make_buffer_rsrc(ws, 0, WS_USED, 0x00020000);
  c.ws = ws; c.sh = (LAS char*)shm; c.shb = (unsigned)(uintptr_t)c.sh;
  c.w = __builtin_amdgcn_readfirstlane(threadIdx.x >> 6); c.lane = threadIdx.x & 63;
  c.x = blockIdx.x & 7; c.j = blockIdx.x >> 3;
  u64 e0 = __hip_atomic_load((GAS u64*)ws + FL_EPOCH, RLX_AGENT);
  const In in{x, wqkv, wo, wg, wu, wd, out};
  const bool cons = c.w < 4;
  prep_first(c, in, e0);
  PIN(e0); set_epoch(c, e0);
#pragma nounroll
  for (int L = 0; L < 2; ++L) {
    if (cons) qkv_consumer(fresh(c), L); else if (L == 0) qkv_streamer<true>(fresh(c), 0, in); else qkv_streamer<false>(fresh(c), 1, in);
    attention(fresh(c), L);
    if (cons) { attention_done<0>(fresh(c), L); wd_consumer<false>(fresh(c), L, out); gu_consumer(fresh(c), L); wd_consumer<true>(fresh(c), L, out); }
    else if (c.w < 6) { u32x4 park[45]; wd_streamer<false, false>(fresh(c), L, park); gu_streamer<false>(fresh(c), L, park); wd_streamer<true, false>(fresh(c), L, park); }
    else { u32x4 park[45]; wd_streamer<false, true>(fresh(c), L, park); gu_streamer<true>(fresh(c), L, park); wd_streamer<true, true>(fresh(c), L, park); }
  }
  if (blockIdx.x == 0 && threadIdx.x == 0) __hip_atomic_store((GAS u64*)ws + FL_EPOCH, c.ep, RLX_AGENT);
}

extern "C" void kernel_launch(void* const* d_in, const int* in_sizes, int n_in, void* d_out, int out_size, void* d_ws, size_t ws_size, hipStream_t stream) {
  (void)in_sizes; (void)n_in; (void)out_size; (void)ws_size;
  (void)hipFuncSetAttribute((const void*)fused_kernel, hipFuncAttributeMaxDynamicSharedMemorySize, LDS_BYTES);
  fused_kernel<<<256, 512, LDS_BYTES, stream>>>((const char*)d_in[0], (const char*)d_in[1], (const char*)d_in[2], (const char*)d_in[4], (const char*)d_in[5], (const char*)d_in[6],
                                                (char*)d_ws, (char*)d_out);
}
```
